```python
import torch

def _softmax(x, axis=-1):
    return torch.softmax(x, dim=axis)

def _gelu(x):
    return torch.nn.functional.gelu(x, approximate='tanh')

def _silu(x):
    return torch.nn.functional.silu(x)

def _sigmoid(x):
    return torch.sigmoid(x)

def _layernorm(x, axis=-1, eps=1e-05):
    m = x.mean(dim=axis, keepdim=True)
    v = x.var(dim=axis, keepdim=True, unbiased=False)
    return (x - m) / torch.sqrt(v + eps)

def _scatter_add(tgt, idx, src, axis=0):
    return tgt.clone().index_add_(axis, idx, src)

def _segment_sum(src, seg, n):
    out = torch.zeros((n,) + src.shape[1:], dtype=src.dtype)
    return out.index_add_(0, seg, src)

def _pad(x, pw):
    flat = tuple((v for pair in reversed(pw) for v in pair))
    return torch.nn.functional.pad(x, flat)
kernel_name = 'adaLN_cond__mha_block__dense_activation_silu__prompt'

def setup_inputs():

    _w = lambda a: (lambda t: t.to(torch.bfloat16) if t.is_floating_point() else t)(torch.as_tensor(a))

    def _A_setup():
        B, S, D, H = (1, 2048, 576, 32)
        return {'x': _w((lambda *s: torch.randn(*s, dtype=torch.bfloat16) * 0.05)(B, S, D)), 'wqkv': _w((lambda *s: torch.randn(*s, dtype=torch.bfloat16) * 0.05)(D, 3 * D)), 'wo': _w((lambda *s: torch.randn(*s, dtype=torch.bfloat16) * 0.05)(D, D))}
    _A_in = _A_setup()

    def _B_setup():
        B, S, D, F = (1, 2048, 576, 4608)
        return {'x': _w((lambda *s: torch.randn(*s, dtype=torch.bfloat16) * 0.05)(B, S, D)), 'w1': _w((lambda *s: torch.randn(*s, dtype=torch.bfloat16) * 3.2)(D, F) * D ** (-0.5)), 'w2': _w((lambda *s: torch.randn(*s, dtype=torch.bfloat16) * 0.4)(F, D) * F ** (-0.5))}
    _B_in = _B_setup()
    return {'A_x': _A_in['x'], 'A_wqkv': _A_in['wqkv'], 'A_wo': _A_in['wo'], 'B_x': _B_in['x'], 'B_w1': _B_in['w1'], 'B_w2': _B_in['w2'], 'c': _w((lambda *s: torch.randn(*s, dtype=torch.bfloat16) * 0.1)(1, 576) * 8.0), 'w_mod': _w((lambda *s: torch.randn(*s, dtype=torch.bfloat16) * 0.1)(576, 6 * 576) * 576 ** (-0.5) * 8.0)}

def reference(A_x, A_wqkv, A_wo, B_x, B_w1, B_w2, c, w_mod):

    def _A_ref(x, wqkv, wo):
        B, S, D = x.shape
        H = 32
        Dh = D // H
        qkv = (lambda x, *d: x.permute(*d))((x @ wqkv).reshape(B, S, 3, H, Dh), 2, 0, 3, 1, 4)
        q, k, v = (qkv[0], qkv[1], qkv[2])
        scores = torch.einsum('bhqd,bhkd->bhqk', q, k) * Dh ** (-0.5)
        p = _softmax(scores, axis=-1)
        o = torch.einsum('bhqk,bhkd->bhqd', p, v)
        return (lambda x, *d: x.permute(*d))(o, 0, 2, 1, 3).reshape(B, S, D) @ wo

    def _B_ref(x, w1, w2):
        h = torch.einsum('bsd,df->bsf', x, w1)
        h = _silu(h)
        out = torch.einsum('bsf,fd->bsd', h, w2)
        return out
    x = A_x
    mod = (_silu(c) @ w_mod).reshape(c.shape[0], 6, 1, -1)
    sa, ca, ga, sb, cb, gb = (mod[:, 0].clone(), mod[:, 1].clone(), mod[:, 2].clone(), mod[:, 3].clone(), mod[:, 4].clone(), mod[:, 5].clone())
    h = _layernorm(x, axis=-1) * (1 + ca) + sa
    x = x + ga * _A_ref(h, A_wqkv, A_wo)
    h = _layernorm(x, axis=-1) * (1 + cb) + sb
    x = x + gb * _B_ref(h, B_w1, B_w2)
    return x
```

```cpp
#include <hip/hip_runtime.h>
#include <hip/hip_bf16.h>
#include <stdint.h>

typedef __attribute__((ext_vector_type(8))) __bf16 bf16x8;
typedef __attribute__((ext_vector_type(2))) __bf16 bf16x2;
typedef __attribute__((ext_vector_type(4))) short s16x4;
typedef __attribute__((ext_vector_type(2))) float f32x2;
typedef __attribute__((ext_vector_type(4))) float f32x4;
typedef __attribute__((ext_vector_type(16))) float f32x16;
typedef __attribute__((ext_vector_type(2))) unsigned u32x2;
typedef __attribute__((ext_vector_type(4))) unsigned u32x4;
typedef unsigned short u16;

#define DI __device__ __forceinline__

constexpr int S_ = 2048, D_ = 576, F_ = 4608;
constexpr float LN_EPS = 1e-5f;
constexpr float QSCALE = 0.23570226039551584f * 1.4426950408889634f;

constexpr size_t WS_MOD  = 0;
constexpr size_t WS_STAT = 16384;
constexpr size_t WS_WQKV = 32768;
constexpr size_t WS_QLO  = WS_WQKV + 2359296;
constexpr size_t WS_QHI  = WS_QLO + 2097152;
constexpr int    KV_HEAD = 159744, KV_KHI = 65536, KV_VT = 81920, VT_TILE = 1216;
constexpr size_t WS_KV   = WS_QHI + 262144;
constexpr size_t WS_OT   = WS_KV + 32 * (size_t)KV_HEAD;
constexpr size_t WS_WO   = WS_OT + 2359296;
constexpr size_t WS_W1   = WS_WO + 663552;
constexpr size_t WS_W2   = WS_W1 + 5308416;
constexpr size_t WS_X1   = WS_W2 + 5308416;
constexpr size_t WS_PART = WS_X1 + 2359296;
constexpr size_t WS_END  = WS_PART + 18874368;

DI float bf2f(u16 v) { return __uint_as_float((unsigned)v << 16); }
DI float lo2f(unsigned v) { return __uint_as_float(v << 16); }
DI float hi2f(unsigned v) { return __uint_as_float(v & 0xffff0000u); }
DI unsigned pk2(float a, float b) { bf16x2 v = {(__bf16)a, (__bf16)b}; return __builtin_bit_cast(unsigned, v); }
DI float rbf(float a) { return (float)(__bf16)a; }
DI f32x4 mfma16(u32x4 a, u32x4 b, f32x4 c) {
  return __builtin_amdgcn_mfma_f32_16x16x32_bf16(__builtin_bit_cast(bf16x8, a), __builtin_bit_cast(bf16x8, b), c, 0, 0, 0);
}
DI f32x16 mfma32(u32x4 a, u32x4 b, f32x16 c) {
  return __builtin_amdgcn_mfma_f32_32x32x16_bf16(__builtin_bit_cast(bf16x8, a), __builtin_bit_cast(bf16x8, b), c, 0, 0, 0);
}
DI f32x16 mfma32k8(u32x2 a, u32x2 b, f32x16 c) {
  return __builtin_amdgcn_mfma_f32_32x32x8bf16_1k(__builtin_bit_cast(s16x4, a), __builtin_bit_cast(s16x4, b), c, 0, 0, 0);
}
DI float fast_rcp(float x) { return __builtin_amdgcn_rcpf(x); }
DI float fast_exp2(float x) { return __builtin_amdgcn_exp2f(x); }
DI float silu_f(float x) { return x * fast_rcp(1.f + fast_exp2(-1.4426950408889634f * x)); }
DI float xor_add(float v, int m) { return v + __shfl_xor(v, m); }
DI void glds16(const void* g, unsigned lds_base) {
  unsigned sv;
  asm volatile("s_mov_b32 %0, m0\n\ts_mov_b32 m0, %2\n\ts_nop 0\n\tglobal_load_lds_dwordx4 %1, off\n\ts_mov_b32 m0, %0"
               : "=&s"(sv) : "v"(g), "s"(lds_base) : "memory");
}
template <class KF>
DI u32x4 gather_frag(const u16* __restrict__ src, int ld, int col, bool valid, KF krow) {
  u16 e[8];
#pragma unroll
  for (int j = 0; j < 8; ++j) e[j] = src[(size_t)krow(j) * ld + col];
  u32x4 v;
#pragma unroll
  for (int j = 0; j < 4; ++j) v[j] = valid ? ((unsigned)e[2 * j] | ((unsigned)e[2 * j + 1] << 16)) : 0u;
  return v;
}

__global__ __launch_bounds__(512) void k_prep(const u16* __restrict__ x, const u16* __restrict__ wqkv, const u16* __restrict__ c,
                                              const u16* __restrict__ wmod, char* __restrict__ ws) {
  __shared__ float red[128 * 16];
  const int b = blockIdx.x, tid = threadIdx.x, l = tid & 63;
  const int w = __builtin_amdgcn_readfirstlane(tid >> 6);
  if (w < 4) {
    if (b >= 216) return;
    const int cg = (b & 7) * 27 + (b >> 3);
    const int half = tid & 1, row0 = tid >> 1;
    u32x4 wv[5]; u16 cv[5];
#pragma unroll
    for (int i = 0; i < 5; ++i) {
      const int k = min(row0 + 128 * i, 575);
      wv[i] = *(const u32x4*)(wmod + (size_t)k * 3456 + cg * 16 + half * 8);
      cv[i] = c[k];
    }
    float acc[8];
#pragma unroll
    for (int j = 0; j < 8; ++j) acc[j] = 0.f;
#pragma unroll
    for (int i = 0; i < 5; ++i) {
      float s = rbf(silu_f(bf2f(cv[i])));
      if (row0 + 128 * i >= 576) s = 0.f;
#pragma unroll
      for (int j = 0; j < 4; ++j) { acc[2 * j] += s * lo2f(wv[i][j]); acc[2 * j + 1] += s * hi2f(wv[i][j]); }
    }
    *(f32x4*)&red[row0 * 16 + half * 8] = f32x4{acc[0], acc[1], acc[2], acc[3]};
    *(f32x4*)&red[row0 * 16 + half * 8 + 4] = f32x4{acc[4], acc[5], acc[6], acc[7]};
    __syncthreads();
    if (w == 0) {
      const int col = l & 15, part = l >> 4;
      float s = 0.f;
#pragma unroll
      for (int i = 0; i < 32; ++i) s += red[(part * 32 + i) * 16 + col];
      s = xor_add(s, 16); s = xor_add(s, 32);
      if (l < 16) {
        const int n = cg * 16 + col, sec = n / 576;
        float v = rbf(s);
        if (sec == 1 || sec == 4) v = rbf(1.f + v);
        ((float*)(ws + WS_MOD))[n] = v;
      }
    }
  } else if (w == 4) {
    const int row = 8 * b + (l & 7), sub = l >> 3;
    u32x4 xv[9];
#pragma unroll
    for (int i = 0; i < 9; ++i) xv[i] = *(const u32x4*)(x + (size_t)row * D_ + (sub + 8 * i) * 8);
    float s = 0.f;
#pragma unroll
    for (int i = 0; i < 9; ++i)
#pragma unroll
      for (int j = 0; j < 4; ++j) s += lo2f(xv[i][j]) + hi2f(xv[i][j]);
    s = xor_add(s, 8); s = xor_add(s, 16); s = xor_add(s, 32);
    const float mu = s * (1.f / D_);
    float q = 0.f;
#pragma unroll
    for (int i = 0; i < 9; ++i)
#pragma unroll
      for (int j = 0; j < 4; ++j) { const float a = lo2f(xv[i][j]) - mu, bb = hi2f(xv[i][j]) - mu; q += a * a + bb * bb; }
    q = xor_add(q, 8); q = xor_add(q, 16); q = xor_add(q, 32);
    const float rstd = 1.f / sqrtf(q * (1.f / D_) + LN_EPS);
    if (sub == 0) ((f32x2*)(ws + WS_STAT))[row] = f32x2{rstd, -mu * rstd};
  } else {
    const int xg = b & 7, rank = b >> 3;
    u32x4 fr[3];
#pragma unroll
    for (int i = 0; i < 3; ++i) {
      const int fid = rank * 9 + (w - 5) * 3 + i;
      const int hl = fid / 72, nb = (fid % 72) / 18, ks = fid % 18;
      const int head = 4 * xg + hl, n = l & 15;
      int col; bool valid = true;
      if (nb < 3) col = nb * 576 + head * 18 + n;
      else { valid = n < 6; col = valid ? ((n >> 1) * 576 + head * 18 + 16 + (n & 1)) : 0; }
      const int k0 = ks * 32 + (l >> 4) * 8;
      fr[i] = gather_frag(wqkv, 1728, col, valid, [&](int j) { return k0 + j; });
    }
#pragma unroll
    for (int i = 0; i < 3; ++i) {
      const int fid = rank * 9 + (w - 5) * 3 + i;
      *(u32x4*)(ws + WS_WQKV + (size_t)(xg * 288 + fid) * 1024 + l * 16) = fr[i];
    }
  }
}

template <bool STATS>
DI void ln_tile(const u16* __restrict__ xrows, const f32x2* __restrict__ stat, const float* gs, char* A, int w, int l) {
  const int sub = l >> 3;
  u32x4 xv[2][9];
#pragma unroll
  for (int p = 0; p < 2; ++p)
#pragma unroll
    for (int i = 0; i < 9; ++i)
      xv[p][i] = *(const u32x4*)(xrows + (size_t)(16 * w + 8 * p + (l & 7)) * D_ + (sub + 8 * i) * 8);
  f32x2 st[2];
  if constexpr (!STATS) {
#pragma unroll
    for (int p = 0; p < 2; ++p) st[p] = stat[16 * w + 8 * p + (l & 7)];
  }
  __syncthreads();
#pragma unroll
  for (int p = 0; p < 2; ++p) {
    if constexpr (STATS) {
      float s = 0.f;
#pragma unroll
      for (int i = 0; i < 9; ++i)
#pragma unroll
        for (int j = 0; j < 4; ++j) s += lo2f(xv[p][i][j]) + hi2f(xv[p][i][j]);
      s = xor_add(s, 8); s = xor_add(s, 16); s = xor_add(s, 32);
      const float mu = s * (1.f / D_);
      float q = 0.f;
#pragma unroll
      for (int i = 0; i < 9; ++i)
#pragma unroll
        for (int j = 0; j < 4; ++j) { const float a = lo2f(xv[p][i][j]) - mu, bb = hi2f(xv[p][i][j]) - mu; q += a * a + bb * bb; }
      q = xor_add(q, 8); q = xor_add(q, 16); q = xor_add(q, 32);
      const float rstd = 1.f / sqrtf(q * (1.f / D_) + LN_EPS);
      st[p] = f32x2{rstd, -mu * rstd};
    }
#pragma unroll
    for (int i = 0; i < 9; ++i) {
      const int ch = sub + 8 * i;
      const f32x4 g0 = *(const f32x4*)(gs + ch * 8), g1 = *(const f32x4*)(gs + ch * 8 + 4);
      const f32x4 s0 = *(const f32x4*)(gs + 576 + ch * 8), s1 = *(const f32x4*)(gs + 576 + ch * 8 + 4);
      u32x4 o;
#pragma unroll
      for (int j = 0; j < 4; ++j) {
        const float ga = j < 2 ? g0[2 * j] : g1[2 * j - 4], gb = j < 2 ? g0[2 * j + 1] : g1[2 * j - 3];
        const float sa = j < 2 ? s0[2 * j] : s1[2 * j - 4], sb = j < 2 ? s0[2 * j + 1] : s1[2 * j - 3];
        const float a = (lo2f(xv[p][i][j]) * st[p][0] + st[p][1]) * ga + sa;
        const float bb = (hi2f(xv[p][i][j]) * st[p][0] + st[p][1]) * gb + sb;
        o[j] = pk2(a, bb);
      }
      *(u32x4*)(A + ((w * 18 + (ch >> 2)) * 64 + (ch & 3) * 16 + 8 * p + (l & 7)) * 16) = o;
    }
  }
}

__global__ __launch_bounds__(256, 1) void k_qkv(const u16* __restrict__ x, char* __restrict__ ws) {
  __shared__ __attribute__((aligned(16))) char A[73728];
  __shared__ __attribute__((aligned(16))) float gs[1152];
  const int b = blockIdx.x, tid = threadIdx.x, l = tid & 63;
  const int w = __builtin_amdgcn_readfirstlane(tid >> 6);
  const int xg = b & 7, r = b >> 3;
  const char* wt = ws + WS_WQKV + (size_t)((xg * 4 + w) * 72) * 1024 + l * 16;
  constexpr int PF = 6;
  u32x4 wf[18][4];
#pragma unroll
  for (int ks = 0; ks < PF; ++ks)
#pragma unroll
    for (int nb = 0; nb < 4; ++nb) wf[ks][nb] = *(const u32x4*)(wt + (nb * 18 + ks) * 1024);
  {
    const float* mod = (const float*)(ws + WS_MOD);
    for (int i = tid; i < 576; i += 256) { gs[i] = mod[576 + i]; gs[576 + i] = mod[i]; }
  }
  ln_tile<false>(x + (size_t)r * 64 * D_, (const f32x2*)(ws + WS_STAT) + r * 64, gs, A, w, l);
  __syncthreads();
  f32x4 aq[4], ak[4], av[4], am[4];
#pragma unroll
  for (int mb = 0; mb < 4; ++mb) { aq[mb] = f32x4{0, 0, 0, 0}; ak[mb] = aq[mb]; av[mb] = aq[mb]; am[mb] = aq[mb]; }
#pragma unroll
  for (int ks = 0; ks < 18; ++ks) {
    if (ks + PF < 18) {
#pragma unroll
      for (int nb = 0; nb < 4; ++nb) wf[ks + PF][nb] = *(const u32x4*)(wt + (nb * 18 + ks + PF) * 1024);
    }
#pragma unroll
    for (int mb = 0; mb < 4; ++mb) {
      const u32x4 hf = *(const u32x4*)(A + (mb * 18 + ks) * 1024 + l * 16);
      aq[mb] = mfma16(wf[ks][0], hf, aq[mb]);
      ak[mb] = mfma16(wf[ks][1], hf, ak[mb]);
      av[mb] = mfma16(hf, wf[ks][2], av[mb]);
      am[mb] = mfma16(wf[ks][3], hf, am[mb]);
    }
  }
  const int head = 4 * xg + w, g = l >> 4, n = l & 15;
  char* kvh = ws + WS_KV + (size_t)head * KV_HEAD;
  char* qlo = ws + WS_QLO + (size_t)head * 65536;
  unsigned* qhi = (unsigned*)(ws + WS_QHI) + head * 2048;
#pragma unroll
  for (int mb = 0; mb < 4; ++mb) {
    const int tok0 = r * 64 + mb * 16, t = tok0 >> 5, s = mb & 1, key = 16 * s + n;
    const int off = t * 1024 + (g >> 1) * 512 + key * 16 + (g & 1) * 8;
    *(u32x2*)(qlo + off) = u32x2{pk2(aq[mb][0] * QSCALE, aq[mb][1] * QSCALE), pk2(aq[mb][2] * QSCALE, aq[mb][3] * QSCALE)};
    *(u32x2*)(kvh + off) = u32x2{pk2(ak[mb][0], ak[mb][1]), pk2(ak[mb][2], ak[mb][3])};
    char* vt = kvh + KV_VT + t * VT_TILE + s * 608;
    *(u32x2*)(vt + (g & 1) * 304 + n * 16 + (g >> 1) * 8) = u32x2{pk2(av[mb][0], av[mb][1]), pk2(av[mb][2], av[mb][3])};
    const int voff = ((n >> 2) & 1) * 304 + (4 * (n >> 3) + (n & 3)) * 2;
    if (g == 0) {
      qhi[tok0 + n] = pk2(am[mb][0] * QSCALE, am[mb][1] * QSCALE);
      *(u32x2*)(kvh + KV_KHI + (tok0 + n) * 8) = u32x2{pk2(am[mb][2], am[mb][3]), 0x00003f80u};
    } else if (g == 1) {
      const unsigned v = pk2(am[mb][0], am[mb][1]);
      *(u16*)(vt + voff + 16 * 16) = (u16)v;
      *(u16*)(vt + voff + 17 * 16) = (u16)(v >> 16);
    } else if (g == 2) {
      *(u16*)(vt + voff + 18 * 16) = (u16)0x3f80;
    }
  }
}

DI void retile_one(int fid, int xg, int l, const u16* __restrict__ wo, const u16* __restrict__ w1, const u16* __restrict__ w2,
                   char* __restrict__ ws, u32x4& v, char*& dst) {
  const int n = l & 15, kq = l >> 4;
  if (fid < 648) {
    const int wv = fid / 162, nb = (fid % 162) / 18, ks = fid % 18;
    const int col = xg * 576 + wv * 144 + nb * 16 + n, k0 = ks * 32 + kq * 8;
    v = gather_frag(w1, F_, col, true, [&](int j) { return k0 + j; });
    dst = ws + WS_W1 + (size_t)(xg * 648 + fid) * 1024 + l * 16;
  } else if (fid < 1296) {
    const int f2 = fid - 648, wv = f2 / 162, nb = (f2 % 162) / 18, ks = f2 % 18;
    const int col = wv * 144 + (nb < 8 ? 32 * (nb >> 1) + 8 * (n >> 2) + 4 * (nb & 1) + (n & 3) : 128 + n);
    const int fb = xg * 576;
    v = gather_frag(w2, D_, col, true, [&](int j) {
      return ks < 16 ? fb + (ks >> 2) * 144 + (2 * (ks & 3) + (j >> 2)) * 16 + 4 * kq + (j & 3)
                     : fb + (2 * (ks - 16) + (j >> 2)) * 144 + 128 + 4 * kq + (j & 3); });
    dst = ws + WS_W2 + (size_t)(xg * 648 + f2) * 1024 + l * 16;
  } else {
    const int f3 = xg * 81 + (fid - 1296), nb = f3 / 18, ks = f3 % 18;
    const int bi = 4 * ks + kq, xx = bi / 9, u = bi % 9;
    v = gather_frag(wo, D_, nb * 16 + n, true, [&](int j) {
      return u < 8 ? (4 * xx + (u >> 1)) * 18 + 8 * (u & 1) + j : (4 * xx + (j >> 1)) * 18 + 16 + (j & 1); });
    dst = ws + WS_WO + (size_t)f3 * 1024 + l * 16;
  }
}

__global__ __launch_bounds__(768) void k_attn(const u16* __restrict__ wo, const u16* __restrict__ w1, const u16* __restrict__ w2,
                                              char* __restrict__ ws) {
  extern __shared__ __attribute__((aligned(16))) char lds[];
  const int b = blockIdx.x, tid = threadIdx.x, l = tid & 63;
  const int w = __builtin_amdgcn_readfirstlane(tid >> 6);
  const int xg = b & 7;
  if (w >= 8) {
    const int id = (b >> 3) * 4 + (w - 8);
    for (int f0 = id; f0 < 1377; f0 += 128 * 4) {
      u32x4 v[4]; char* dst[4];
#pragma unroll
      for (int i = 0; i < 4; ++i) { const int fid = min(f0 + 128 * i, 1376); retile_one(fid, xg, l, wo, w1, w2, ws, v[i], dst[i]); }
#pragma unroll
      for (int i = 0; i < 4; ++i) if (f0 + 128 * i < 1377) *(u32x4*)dst[i] = v[i];
    }
    return;
  }
  const int hl = (b >> 3) & 3, head = 4 * xg + hl, qb = b >> 5;
  const int r32 = l & 31, h = l >> 5;
  const char* kvh = ws + WS_KV + (size_t)head * KV_HEAD;
  const int tq = qb * 8 + w;
  u32x4 qlo; unsigned qh0;
  {
    const char* pq = ws + WS_QLO + (size_t)head * 65536 + tq * 1024 + l * 16;
    const char* ph = ws + WS_QHI + (size_t)head * 8192 + (tq * 32 + r32) * 4;
    asm volatile("global_load_dwordx4 %0, %1, off" : "=v"(qlo) : "v"(pq) : "memory");
    asm volatile("global_load_dword %0, %1, off" : "=v"(qh0) : "v"(ph) : "memory");
  }
  const unsigned lds0 = (unsigned)(uintptr_t)lds;
#pragma unroll
  for (int G = 0; G < 4; ++G)
#pragma unroll
    for (int m = 0; m < 5; ++m) {
      const int i = min(w + 8 * m, 38);
      const int off = i < 16 ? (G * 16 + i) * 1024 : (i < 20 ? KV_KHI + G * 4096 + (i - 16) * 1024 : KV_VT + G * 19456 + (i - 20) * 1024);
      glds16(kvh + off + l * 16, (unsigned)__builtin_amdgcn_readfirstlane(lds0 + off));
    }
  asm volatile("s_waitcnt vmcnt(15)\n\ts_barrier" : "+v"(qlo), "+v"(qh0) :: "memory");
  const char* pk_lo = lds + l * 16;
  const char* pk_hi = lds + KV_KHI + r32 * 8;
  const char* pv = lds + KV_VT + h * 304 + min(r32, 18) * 16;
  u32x2 qhi = u32x2{h == 0 ? qh0 : 0u, 0u};
  const f32x16 zero16 = {0, 0, 0, 0, 0, 0, 0, 0, 0, 0, 0, 0, 0, 0, 0, 0};
  {
    f32x16 s0 = mfma32(*(const u32x4*)pk_lo, qlo, zero16);
    s0 = mfma32k8(*(const u32x2*)pk_hi, qhi, s0);
    float m = s0[0];
#pragma unroll
    for (int i = 1; i < 16; ++i) m = fmaxf(m, s0[i]);
    auto rr = __builtin_amdgcn_permlane32_swap(__float_as_uint(m), __float_as_uint(m), false, false);
    m = fmaxf(__uint_as_float(rr[0]), __uint_as_float(rr[1]));
    qhi[1] = h == 0 ? (pk2(-m, 0.f) & 0xffffu) : 0u;
  }
  f32x16 o = zero16;
#pragma unroll
  for (int G = 0; G < 4; ++G) {
    if (G == 1) asm volatile("s_waitcnt vmcnt(10)\n\ts_barrier" ::: "memory");
    if (G == 2) asm volatile("s_waitcnt vmcnt(5)\n\ts_barrier" ::: "memory");
    if (G == 3) asm volatile("s_waitcnt vmcnt(0)\n\ts_barrier" ::: "memory");
#pragma unroll 2
    for (int tt = 0; tt < 16; ++tt) {
      const int t = G * 16 + tt;
      const u32x4 kl = *(const u32x4*)(pk_lo + t * 1024);
      const u32x2 kh = *(const u32x2*)(pk_hi + t * 256);
      const u32x4 v0 = *(const u32x4*)(pv + t * VT_TILE);
      const u32x4 v1 = *(const u32x4*)(pv + t * VT_TILE + 608);
      f32x16 s = mfma32(kl, qlo, zero16);
      s = mfma32k8(kh, qhi, s);
      u32x4 p0, p1;
#pragma unroll
      for (int j = 0; j < 4; ++j) {
        p0[j] = pk2(fast_exp2(s[2 * j]), fast_exp2(s[2 * j + 1]));
        p1[j] = pk2(fast_exp2(s[8 + 2 * j]), fast_exp2(s[8 + 2 * j + 1]));
      }
      o = mfma32(v0, p0, o);
      o = mfma32(v1, p1, o);
    }
  }
  {
    bool bad = false;
#pragma unroll
    for (int i = 0; i < 11; ++i) bad |= !(fabsf(o[i]) < 3.0e38f);
    if (__any(bad)) {
      qhi[1] = 0u;
      float m = -3.0e38f;
      o = zero16;
      for (int t = 0; t < 64; ++t) {
        const u32x4 kl = *(const u32x4*)(pk_lo + t * 1024);
        const u32x2 kh = *(const u32x2*)(pk_hi + t * 256);
        const u32x4 v0 = *(const u32x4*)(pv + t * VT_TILE);
        const u32x4 v1 = *(const u32x4*)(pv + t * VT_TILE + 608);
        f32x16 s = mfma32(kl, qlo, zero16);
        s = mfma32k8(kh, qhi, s);
        float tm = s[0];
#pragma unroll
        for (int i = 1; i < 16; ++i) tm = fmaxf(tm, s[i]);
        auto rr = __builtin_amdgcn_permlane32_swap(__float_as_uint(tm), __float_as_uint(tm), false, false);
        tm = fmaxf(__uint_as_float(rr[0]), __uint_as_float(rr[1]));
        const float mn = fmaxf(m, tm), al = fast_exp2(m - mn);
        m = mn;
#pragma unroll
        for (int i = 0; i < 16; ++i) o[i] *= al;
        u32x4 p0, p1;
#pragma unroll
        for (int j = 0; j < 4; ++j) {
          p0[j] = pk2(fast_exp2(s[2 * j] - m), fast_exp2(s[2 * j + 1] - m));
          p1[j] = pk2(fast_exp2(s[8 + 2 * j] - m), fast_exp2(s[8 + 2 * j + 1] - m));
        }
        o = mfma32(v0, p0, o);
        o = mfma32(v1, p1, o);
      }
    }
  }
  const float inv = 1.f / o[10];
  const unsigned a0 = pk2(o[0] * inv, o[1] * inv), a1 = pk2(o[2] * inv, o[3] * inv);
  const unsigned b0 = pk2(o[4] * inv, o[5] * inv), b1 = pk2(o[6] * inv, o[7] * inv);
  const unsigned tail = pk2(o[8] * inv, o[9] * inv);
  auto r0 = __builtin_amdgcn_permlane32_swap(a0, b0, false, false);
  auto r1 = __builtin_amdgcn_permlane32_swap(a1, b1, false, false);
  const int tok = qb * 256 + w * 32 + r32, mb = tok >> 4, row = tok & 15;
  char* ot = ws + WS_OT + (size_t)mb * 18432 + row * 16;
  const int bi = xg * 9 + 2 * hl + h, bt = xg * 9 + 8;
  *(u32x4*)(ot + (bi >> 2) * 1024 + (bi & 3) * 256) = u32x4{r0[0], r1[0], r0[1], r1[1]};
  if (h == 0) *(unsigned*)(ot + (bt >> 2) * 1024 + (bt & 3) * 256 + hl * 4) = tail;
}

__global__ __launch_bounds__(192) void k_wo(const u16* __restrict__ x, char* __restrict__ ws) {
  __shared__ __attribute__((aligned(16))) char A[36864];
  const int b = blockIdx.x, tid = threadIdx.x, l = tid & 63;
  const int w = __builtin_amdgcn_readfirstlane(tid >> 6);
  const int cb = b & 3, rb = b >> 2;
  const char* src = ws + WS_OT + (size_t)rb * 36864;
  u32x4 av[12];
#pragma unroll
  for (int i = 0; i < 12; ++i) av[i] = *(const u32x4*)(src + (w * 12 + i) * 1024 + l * 16);
  const char* wt = ws + WS_WO + (size_t)((cb * 9 + 3 * w) * 18) * 1024 + l * 16;
  u32x4 wf[18][3];
#pragma unroll
  for (int ks = 0; ks < 18; ++ks)
#pragma unroll
    for (int nb = 0; nb < 3; ++nb) wf[ks][nb] = *(const u32x4*)(wt + (nb * 18 + ks) * 1024);
#pragma unroll
  for (int i = 0; i < 12; ++i) *(u32x4*)(A + (w * 12 + i) * 1024 + l * 16) = av[i];
  __syncthreads();
  f32x4 acc[2][3];
#pragma unroll
  for (int mb = 0; mb < 2; ++mb)
#pragma unroll
    for (int nb = 0; nb < 3; ++nb) acc[mb][nb] = f32x4{0, 0, 0, 0};
#pragma unroll
  for (int ks = 0; ks < 18; ++ks)
#pragma unroll
    for (int mb = 0; mb < 2; ++mb) {
      const u32x4 af = *(const u32x4*)(A + (mb * 18 + ks) * 1024 + l * 16);
#pragma unroll
      for (int nb = 0; nb < 3; ++nb) acc[mb][nb] = mfma16(wf[ks][nb], af, acc[mb][nb]);
    }
  const float* ga = (const float*)(ws + WS_MOD) + 2 * 576;
  u16* x1 = (u16*)(ws + WS_X1);
  const int g = l >> 4, n = l & 15;
#pragma unroll
  for (int mb = 0; mb < 2; ++mb)
#pragma unroll
    for (int nb = 0; nb < 3; ++nb) {
      const int tok = rb * 32 + mb * 16 + n, col = (cb * 9 + 3 * w + nb) * 16 + 4 * g;
      const u32x2 xv = *(const u32x2*)(x + (size_t)tok * D_ + col);
      const f32x4 gv = *(const f32x4*)(ga + col);
      const f32x4 a = acc[mb][nb];
      *(u32x2*)(x1 + (size_t)tok * D_ + col) =
          u32x2{pk2(lo2f(xv[0]) + gv[0] * a[0], hi2f(xv[0]) + gv[1] * a[1]), pk2(lo2f(xv[1]) + gv[2] * a[2], hi2f(xv[1]) + gv[3] * a[3])};
    }
}

template <int PF>
DI void gemm_64x144(const char* __restrict__ wt, const char* A, int l, u32x4 (&wf)[18][9], f32x4 (&acc)[4][9]) {
#pragma unroll
  for (int mb = 0; mb < 4; ++mb)
#pragma unroll
    for (int nb = 0; nb < 9; ++nb) acc[mb][nb] = f32x4{0, 0, 0, 0};
#pragma unroll
  for (int ks = 0; ks < 18; ++ks) {
    if (ks + PF < 18) {
#pragma unroll
      for (int nb = 0; nb < 9; ++nb) wf[ks + PF][nb] = *(const u32x4*)(wt + (nb * 18 + ks + PF) * 1024);
    }
#pragma unroll
    for (int mb = 0; mb < 4; ++mb) {
      const u32x4 hf = *(const u32x4*)(A + (mb * 18 + ks) * 1024 + l * 16);
#pragma unroll
      for (int nb = 0; nb < 9; ++nb) acc[mb][nb] = mfma16(wf[ks][nb], hf, acc[mb][nb]);
    }
  }
}

__global__ __launch_bounds__(256, 1) void k_ffn(char* __restrict__ ws) {
  __shared__ __attribute__((aligned(16))) char A[73728];
  __shared__ __attribute__((aligned(16))) float gs[1152];
  const int b = blockIdx.x, tid = threadIdx.x, l = tid & 63;
  const int w = __builtin_amdgcn_readfirstlane(tid >> 6);
  const int xg = b & 7, r = b >> 3;
  const char* w1t = ws + WS_W1 + (size_t)((xg * 4 + w) * 162) * 1024 + l * 16;
  const char* w2t = ws + WS_W2 + (size_t)((xg * 4 + w) * 162) * 1024 + l * 16;
  constexpr int PF = 3;
  u32x4 wf[18][9];
#pragma unroll
  for (int ks = 0; ks < PF; ++ks)
#pragma unroll
    for (int nb = 0; nb < 9; ++nb) wf[ks][nb] = *(const u32x4*)(w1t + (nb * 18 + ks) * 1024);
  {
    const float* mod = (const float*)(ws + WS_MOD);
    for (int i = tid; i < 576; i += 256) { gs[i] = mod[4 * 576 + i]; gs[576 + i] = mod[3 * 576 + i]; }
  }
  ln_tile<true>((const u16*)(ws + WS_X1) + (size_t)r * 64 * D_, nullptr, gs, A, w, l);
  __syncthreads();
  f32x4 acc[4][9];
  gemm_64x144<PF>(w1t, A, l, wf, acc);
#pragma unroll
  for (int ks = 0; ks < PF; ++ks)
#pragma unroll
    for (int nb = 0; nb < 9; ++nb) wf[ks][nb] = *(const u32x4*)(w2t + (nb * 18 + ks) * 1024);
  u32x2 xp[4][9];
#pragma unroll
  for (int mb = 0; mb < 4; ++mb)
#pragma unroll
    for (int nb = 0; nb < 9; ++nb)
      xp[mb][nb] = u32x2{pk2(silu_f(acc[mb][nb][0]), silu_f(acc[mb][nb][1])), pk2(silu_f(acc[mb][nb][2]), silu_f(acc[mb][nb][3]))};
  __syncthreads();
#pragma unroll
  for (int mb = 0; mb < 4; ++mb) {
#pragma unroll
    for (int p = 0; p < 4; ++p)
      *(u32x4*)(A + (mb * 18 + w * 4 + p) * 1024 + l * 16) = u32x4{xp[mb][2 * p][0], xp[mb][2 * p][1], xp[mb][2 * p + 1][0], xp[mb][2 * p + 1][1]};
    *(u32x2*)(A + (mb * 18 + 16 + (w >> 1)) * 1024 + l * 16 + (w & 1) * 8) = xp[mb][8];
  }
  __syncthreads();
  gemm_64x144<PF>(w2t, A, l, wf, acc);
  char* part = ws + WS_PART + (size_t)(xg * 32 + r) * 73728 + w * 18432;
#pragma unroll
  for (int mb = 0; mb < 4; ++mb) {
#pragma unroll
    for (int p = 0; p < 4; ++p)
      *(u32x4*)(part + mb * 4608 + p * 1024 + l * 16) =
          u32x4{pk2(acc[mb][2 * p][0], acc[mb][2 * p][1]), pk2(acc[mb][2 * p][2], acc[mb][2 * p][3]),
                pk2(acc[mb][2 * p + 1][0], acc[mb][2 * p + 1][1]), pk2(acc[mb][2 * p + 1][2], acc[mb][2 * p + 1][3])};
    *(u32x2*)(part + mb * 4608 + 4096 + l * 8) = u32x2{pk2(acc[mb][8][0], acc[mb][8][1]), pk2(acc[mb][8][2], acc[mb][8][3])};
  }
}

__global__ __launch_bounds__(256) void k_red(const char* __restrict__ ws, u16* __restrict__ out) {
  const int b = blockIdx.x, tid = threadIdx.x, l = tid & 63;
  const int v = __builtin_amdgcn_readfirstlane(tid >> 6);
  const int r = b >> 3, wm = 2 * (b & 7) + (v >> 1), wv = wm >> 2, mb = wm & 3;
  const char* part = ws + WS_PART + (size_t)r * 73728 + wm * 4608;
  const u16* x1 = (const u16*)(ws + WS_X1);
  const float* gb = (const float*)(ws + WS_MOD) + 5 * 576;
  const int g = l >> 4, tok = r * 64 + mb * 16 + (l & 15);
  u32x4 pv[2][8]; u32x2 sv[8];
#pragma unroll
  for (int i = 0; i < 2; ++i)
#pragma unroll
    for (int xg = 0; xg < 8; ++xg) pv[i][xg] = *(const u32x4*)(part + (size_t)xg * 32 * 73728 + (2 * (v & 1) + i) * 1024 + l * 16);
  if ((v & 1) == 0) {
#pragma unroll
    for (int xg = 0; xg < 8; ++xg) sv[xg] = *(const u32x2*)(part + (size_t)xg * 32 * 73728 + 4096 + l * 8);
  }
#pragma unroll
  for (int i = 0; i < 2; ++i) {
    const int col = wv * 144 + 32 * (2 * (v & 1) + i) + 8 * g;
    const u32x4 xv = *(const u32x4*)(x1 + (size_t)tok * D_ + col);
    const f32x4 g0 = *(const f32x4*)(gb + col), g1 = *(const f32x4*)(gb + col + 4);
    u32x4 o;
#pragma unroll
    for (int j = 0; j < 4; ++j) {
      float a = 0.f, c = 0.f;
#pragma unroll
      for (int xg = 0; xg < 8; ++xg) { a += lo2f(pv[i][xg][j]); c += hi2f(pv[i][xg][j]); }
      const float ga = j < 2 ? g0[2 * j] : g1[2 * j - 4], gc = j < 2 ? g0[2 * j + 1] : g1[2 * j - 3];
      o[j] = pk2(lo2f(xv[j]) + ga * a, hi2f(xv[j]) + gc * c);
    }
    *(u32x4*)(out + (size_t)tok * D_ + col) = o;
  }
  if ((v & 1) == 0) {
    const int col = wv * 144 + 128 + 4 * g;
    const u32x2 xv = *(const u32x2*)(x1 + (size_t)tok * D_ + col);
    const f32x4 g0 = *(const f32x4*)(gb + col);
    u32x2 o;
#pragma unroll
    for (int j = 0; j < 2; ++j) {
      float a = 0.f, c = 0.f;
#pragma unroll
      for (int xg = 0; xg < 8; ++xg) { a += lo2f(sv[xg][j]); c += hi2f(sv[xg][j]); }
      o[j] = pk2(lo2f(xv[j]) + g0[2 * j] * a, hi2f(xv[j]) + g0[2 * j + 1] * c);
    }
    *(u32x2*)(out + (size_t)tok * D_ + col) = o;
  }
}

extern "C" void kernel_launch(void* const* d_in, const int* in_sizes, int n_in, void* d_out, int out_size, void* d_ws, size_t ws_size,
                              hipStream_t stream) {
  const u16* x = (const u16*)d_in[0];
  const u16* wqkv = (const u16*)d_in[1];
  const u16* wo = (const u16*)d_in[2];
  const u16* w1 = (const u16*)d_in[4];
  const u16* w2 = (const u16*)d_in[5];
  const u16* c = (const u16*)d_in[6];
  const u16* wmod = (const u16*)d_in[7];
  char* ws = (char*)d_ws;
  (void)hipFuncSetAttribute((const void*)k_attn, hipFuncAttributeMaxDynamicSharedMemorySize, KV_HEAD);
  k_prep<<<256, 512, 0, stream>>>(x, wqkv, c, wmod, ws);
  k_qkv<<<256, 256, 0, stream>>>(x, ws);
  k_attn<<<256, 768, KV_HEAD, stream>>>(wo, w1, w2, ws);
  k_wo<<<256, 192, 0, stream>>>(x, ws);
  k_ffn<<<256, 256, 0, stream>>>(ws);
  k_red<<<256, 256, 0, stream>>>(ws, (u16*)d_out);
}
```

```cpp
#include <hip/hip_runtime.h>
#include <hip/hip_bf16.h>
#include <stdint.h>
#include <type_traits>

typedef __attribute__((ext_vector_type(8))) __bf16 bf16x8;
typedef __attribute__((ext_vector_type(2))) __bf16 bf16x2;
typedef __attribute__((ext_vector_type(4))) short s16x4;
typedef __attribute__((ext_vector_type(2))) float f32x2;
typedef __attribute__((ext_vector_type(4))) float f32x4;
typedef __attribute__((ext_vector_type(16))) float f32x16;
typedef __attribute__((ext_vector_type(2))) unsigned u32x2;
typedef __attribute__((ext_vector_type(4))) unsigned u32x4;
typedef unsigned short u16;

#define DI __device__ __forceinline__

constexpr int S_ = 2048, D_ = 576, F_ = 4608;
constexpr float LN_EPS = 1e-5f;
constexpr float QSCALE = 0.23570226039551584f * 1.4426950408889634f;

constexpr size_t WS_MOD  = 0;
constexpr size_t WS_STAT = 16384;
constexpr size_t WS_WQKV = 32768;
constexpr size_t WS_QLO  = WS_WQKV + 2359296;
constexpr size_t WS_QHI  = WS_QLO + 2097152;
constexpr int    KV_HEAD = 159744, KV_KHI = 65536, KV_VT = 81920, VT_TILE = 1216;
constexpr size_t WS_KV   = WS_QHI + 262144;
constexpr size_t WS_OT   = WS_KV + 32 * (size_t)KV_HEAD;
constexpr size_t WS_WO   = WS_OT + 2359296;
constexpr size_t WS_W12  = WS_WO + 663552;
constexpr size_t WS_X1   = WS_W12 + 2 * 5308416;
constexpr size_t WS_PART = WS_X1 + 2359296;
constexpr size_t WS_END  = WS_PART + 18874368;

DI float bf2f(u16 v) { return __uint_as_float((unsigned)v << 16); }
DI float lo2f(unsigned v) { return __uint_as_float(v << 16); }
DI float hi2f(unsigned v) { return __uint_as_float(v & 0xffff0000u); }
DI unsigned pk2(float a, float b) { bf16x2 v = {(__bf16)a, (__bf16)b}; return __builtin_bit_cast(unsigned, v); }
DI float rbf(float a) { return (float)(__bf16)a; }
DI f32x4 mfma16(u32x4 a, u32x4 b, f32x4 c) {
  return __builtin_amdgcn_mfma_f32_16x16x32_bf16(__builtin_bit_cast(bf16x8, a), __builtin_bit_cast(bf16x8, b), c, 0, 0, 0);
}
DI f32x16 mfma32(u32x4 a, u32x4 b, f32x16 c) {
  return __builtin_amdgcn_mfma_f32_32x32x16_bf16(__builtin_bit_cast(bf16x8, a), __builtin_bit_cast(bf16x8, b), c, 0, 0, 0);
}
DI f32x16 mfma32k8(u32x2 a, u32x2 b, f32x16 c) {
  return __builtin_amdgcn_mfma_f32_32x32x8bf16_1k(__builtin_bit_cast(s16x4, a), __builtin_bit_cast(s16x4, b), c, 0, 0, 0);
}
DI float fast_rcp(float x) { return __builtin_amdgcn_rcpf(x); }
DI float fast_exp2(float x) { return __builtin_amdgcn_exp2f(x); }
DI float silu_f(float x) { return x * fast_rcp(1.f + fast_exp2(-1.4426950408889634f * x)); }
DI float xor_add(float v, int m) { return v + __shfl_xor(v, m); }
DI void glds16(const void* g, unsigned lds_base) {
  unsigned sv;
  asm volatile("s_mov_b32 %0, m0\n\ts_mov_b32 m0, %2\n\ts_nop 0\n\tglobal_load_lds_dwordx4 %1, off\n\ts_mov_b32 m0, %0"
               : "=&s"(sv) : "v"(g), "s"(lds_base) : "memory");
}
struct Unit { const u16* src; int ld, a, b; char* pa; char* pb; };
DI void unit_load(const Unit& u, u32x4 (&in)[8]) {
#pragma unroll
  for (int j = 0; j < 8; ++j) in[j] = *(const u32x4*)(u.src + (size_t)(u.a * (j >> 2) + u.b * ((j >> 1) & 1) + (j & 1)) * u.ld);
}
DI void unit_store(const Unit& u, const u32x4 (&in)[8]) {
#pragma unroll
  for (int i = 0; i < 8; ++i) {
    u32x4 o;
#pragma unroll
    for (int m = 0; m < 4; ++m) o[m] = __builtin_amdgcn_perm(in[2 * m + 1][i >> 1], in[2 * m][i >> 1], (i & 1) ? 0x07060302u : 0x05040100u);
    *(u32x4*)((i < 4 ? u.pa : u.pb) + (i & 3) * 16) = o;
  }
}
#define SB() __builtin_amdgcn_sched_barrier(0)

__global__ __launch_bounds__(448) void k_prep(const u16* __restrict__ x, const u16* __restrict__ wqkv, const u16* __restrict__ c,
                                              const u16* __restrict__ wmod, char* __restrict__ ws) {
  __shared__ float red[128 * 16];
  const int b = blockIdx.x, tid = threadIdx.x, l = tid & 63;
  const int w = __builtin_amdgcn_readfirstlane(tid >> 6);
  if (w < 4) {
    if (b >= 216) return;
    const int cg = (b & 7) * 27 + (b >> 3);
    const int half = tid & 1, row0 = tid >> 1;
    u32x4 wv[5]; u16 cv[5];
#pragma unroll
    for (int i = 0; i < 5; ++i) {
      const int k = min(row0 + 128 * i, 575);
      wv[i] = *(const u32x4*)(wmod + (size_t)k * 3456 + cg * 16 + half * 8);
      cv[i] = c[k];
    }
    float acc[8];
#pragma unroll
    for (int j = 0; j < 8; ++j) acc[j] = 0.f;
#pragma unroll
    for (int i = 0; i < 5; ++i) {
      float s = rbf(silu_f(bf2f(cv[i])));
      if (row0 + 128 * i >= 576) s = 0.f;
#pragma unroll
      for (int j = 0; j < 4; ++j) { acc[2 * j] += s * lo2f(wv[i][j]); acc[2 * j + 1] += s * hi2f(wv[i][j]); }
    }
    *(f32x4*)&red[row0 * 16 + half * 8] = f32x4{acc[0], acc[1], acc[2], acc[3]};
    *(f32x4*)&red[row0 * 16 + half * 8 + 4] = f32x4{acc[4], acc[5], acc[6], acc[7]};
    __syncthreads();
    if (w == 0) {
      const int col = l & 15, part = l >> 4;
      float s = 0.f;
#pragma unroll
      for (int i = 0; i < 32; ++i) s += red[(part * 32 + i) * 16 + col];
      s = xor_add(s, 16); s = xor_add(s, 32);
      if (l < 16) {
        const int n = cg * 16 + col, sec = n / 576;
        float v = rbf(s);
        if (sec == 1 || sec == 4) v = rbf(1.f + v);
        ((float*)(ws + WS_MOD))[n] = v;
      }
    }
  } else if (w == 4) {
    const int row = 8 * b + (l & 7), sub = l >> 3;
    u32x4 xv[9];
#pragma unroll
    for (int i = 0; i < 9; ++i) xv[i] = *(const u32x4*)(x + (size_t)row * D_ + (sub + 8 * i) * 8);
    float s = 0.f;
#pragma unroll
    for (int i = 0; i < 9; ++i)
#pragma unroll
      for (int j = 0; j < 4; ++j) s += lo2f(xv[i][j]) + hi2f(xv[i][j]);
    s = xor_add(s, 8); s = xor_add(s, 16); s = xor_add(s, 32);
    const float mu = s * (1.f / D_);
    float q = 0.f;
#pragma unroll
    for (int i = 0; i < 9; ++i)
#pragma unroll
      for (int j = 0; j < 4; ++j) { const float a = lo2f(xv[i][j]) - mu, bb = hi2f(xv[i][j]) - mu; q += a * a + bb * bb; }
    q = xor_add(q, 8); q = xor_add(q, 16); q = xor_add(q, 32);
    const float rstd = 1.f / sqrtf(q * (1.f / D_) + LN_EPS);
    if (sub == 0) ((f32x2*)(ws + WS_STAT))[row] = f32x2{rstd, -mu * rstd};
  } else {
    const int xg = b & 7, rank = b >> 3;
    char* dst = ws + WS_WQKV + (size_t)xg * 288 * 1024;
    if (w == 5) {
      if (l < 54) {
        const int i = rank * 54 + l, kg = i / 24, rem = i % 24, part = rem >> 3, hl = (rem >> 1) & 3, half = rem & 1;
        Unit u;
        u.src = wqkv + (size_t)kg * 8 * 1728 + part * 576 + (4 * xg + hl) * 18 + half * 8;
        u.ld = 1728; u.a = 4; u.b = 2;
        u.pa = dst + ((hl * 18 + (kg >> 2)) * 4 + part) * 1024 + ((kg & 3) * 16 + half * 8) * 16;
        u.pb = u.pa + 64;
        u32x4 in[8];
        unit_load(u, in);
        unit_store(u, in);
      }
    } else if (w == 6) {
      if (l < 9) {
        const int i = rank * 9 + l, kg = i >> 2, hl = i & 3;
        const u16* src = wqkv + (size_t)kg * 8 * 1728 + (4 * xg + hl) * 18 + 16;
        unsigned in[3][8];
#pragma unroll
        for (int p = 0; p < 3; ++p)
#pragma unroll
          for (int j = 0; j < 8; ++j) in[p][j] = *(const unsigned*)(src + (size_t)j * 1728 + p * 576);
        char* pa = dst + ((hl * 18 + (kg >> 2)) * 4 + 3) * 1024 + (kg & 3) * 256;
#pragma unroll
        for (int p = 0; p < 3; ++p)
#pragma unroll
          for (int e = 0; e < 2; ++e) {
            u32x4 o;
#pragma unroll
            for (int m = 0; m < 4; ++m) o[m] = __builtin_amdgcn_perm(in[p][2 * m + 1], in[p][2 * m], e ? 0x07060302u : 0x05040100u);
            *(u32x4*)(pa + (2 * p + e) * 16) = o;
          }
#pragma unroll
        for (int i2 = 6; i2 < 16; ++i2) *(u32x4*)(pa + i2 * 16) = u32x4{0, 0, 0, 0};
      }
    }
  }
}

template <bool STATS>
DI void ln_tile(const u16* __restrict__ xrows, const f32x2* __restrict__ stat, const float* gs, char* A, int w, int l) {
  const int sub = l >> 3;
  u32x4 xv[2][9];
#pragma unroll
  for (int p = 0; p < 2; ++p)
#pragma unroll
    for (int i = 0; i < 9; ++i)
      xv[p][i] = *(const u32x4*)(xrows + (size_t)(16 * w + 8 * p + (l & 7)) * D_ + (sub + 8 * i) * 8);
  f32x2 st[2];
  if constexpr (!STATS) {
#pragma unroll
    for (int p = 0; p < 2; ++p) st[p] = stat[16 * w + 8 * p + (l & 7)];
  }
  __syncthreads();
#pragma unroll
  for (int p = 0; p < 2; ++p) {
    if constexpr (STATS) {
      float s = 0.f;
#pragma unroll
      for (int i = 0; i < 9; ++i)
#pragma unroll
        for (int j = 0; j < 4; ++j) s += lo2f(xv[p][i][j]) + hi2f(xv[p][i][j]);
      s = xor_add(s, 8); s = xor_add(s, 16); s = xor_add(s, 32);
      const float mu = s * (1.f / D_);
      float q = 0.f;
#pragma unroll
      for (int i = 0; i < 9; ++i)
#pragma unroll
        for (int j = 0; j < 4; ++j) { const float a = lo2f(xv[p][i][j]) - mu, bb = hi2f(xv[p][i][j]) - mu; q += a * a + bb * bb; }
      q = xor_add(q, 8); q = xor_add(q, 16); q = xor_add(q, 32);
      const float rstd = 1.f / sqrtf(q * (1.f / D_) + LN_EPS);
      st[p] = f32x2{rstd, -mu * rstd};
    }
#pragma unroll
    for (int i = 0; i < 9; ++i) {
      const int ch = sub + 8 * i;
      const f32x4 g0 = *(const f32x4*)(gs + ch * 8), g1 = *(const f32x4*)(gs + ch * 8 + 4);
      const f32x4 s0 = *(const f32x4*)(gs + 576 + ch * 8), s1 = *(const f32x4*)(gs + 576 + ch * 8 + 4);
      u32x4 o;
#pragma unroll
      for (int j = 0; j < 4; ++j) {
        const float ga = j < 2 ? g0[2 * j] : g1[2 * j - 4], gb = j < 2 ? g0[2 * j + 1] : g1[2 * j - 3];
        const float sa = j < 2 ? s0[2 * j] : s1[2 * j - 4], sb = j < 2 ? s0[2 * j + 1] : s1[2 * j - 3];
        const float a = (lo2f(xv[p][i][j]) * st[p][0] + st[p][1]) * ga + sa;
        const float bb = (hi2f(xv[p][i][j]) * st[p][0] + st[p][1]) * gb + sb;
        o[j] = pk2(a, bb);
      }
      *(u32x4*)(A + ((w * 18 + (ch >> 2)) * 64 + (ch & 3) * 16 + 8 * p + (l & 7)) * 16) = o;
    }
  }
}

__global__ __launch_bounds__(256, 1) void k_qkv(const u16* __restrict__ x, char* __restrict__ ws) {
  __shared__ __attribute__((aligned(16))) char A[73728];
  __shared__ __attribute__((aligned(16))) float gs[1152];
  const int b = blockIdx.x, tid = threadIdx.x, l = tid & 63;
  const int w = __builtin_amdgcn_readfirstlane(tid >> 6);
  const int xg = b & 7, r = b >> 3;
  const char* wt = ws + WS_WQKV + (size_t)((xg * 4 + w) * 72) * 1024 + l * 16;
  constexpr int NS = 6;
  u32x4 wf[NS][4];
#pragma unroll
  for (int ks = 0; ks < NS; ++ks)
#pragma unroll
    for (int nb = 0; nb < 4; ++nb) wf[ks][nb] = *(const u32x4*)(wt + (ks * 4 + nb) * 1024);
  SB();
  {
    const float* mod = (const float*)(ws + WS_MOD);
    for (int i = tid; i < 576; i += 256) { gs[i] = mod[576 + i]; gs[576 + i] = mod[i]; }
  }
  ln_tile<false>(x + (size_t)r * 64 * D_, (const f32x2*)(ws + WS_STAT) + r * 64, gs, A, w, l);
  __syncthreads();
  f32x4 aq[4], ak[4], av[4], am[4];
#pragma unroll
  for (int mb = 0; mb < 4; ++mb) { aq[mb] = f32x4{0, 0, 0, 0}; ak[mb] = aq[mb]; av[mb] = aq[mb]; am[mb] = aq[mb]; }
  u32x4 hf[2][4];
#pragma unroll
  for (int mb = 0; mb < 4; ++mb) hf[0][mb] = *(const u32x4*)(A + mb * 18432 + l * 16);
#pragma unroll
  for (int k0 = 0; k0 < 18; k0 += NS) {
#pragma unroll
    for (int kk = 0; kk < NS; ++kk) {
      const int ks = k0 + kk;
#pragma unroll
      for (int mb = 0; mb < 4; ++mb) {
        if (ks + 1 < 18) hf[(kk + 1) & 1][mb] = *(const u32x4*)(A + (mb * 18 + ks + 1) * 1024 + l * 16);
        aq[mb] = mfma16(wf[kk][0], hf[kk & 1][mb], aq[mb]);
        ak[mb] = mfma16(wf[kk][1], hf[kk & 1][mb], ak[mb]);
        av[mb] = mfma16(hf[kk & 1][mb], wf[kk][2], av[mb]);
        am[mb] = mfma16(wf[kk][3], hf[kk & 1][mb], am[mb]);
        SB();
      }
      if (ks + NS < 18) {
#pragma unroll
        for (int nb = 0; nb < 4; ++nb) wf[kk][nb] = *(const u32x4*)(wt + ((ks + NS) * 4 + nb) * 1024);
      }
      SB();
    }
  }
  const int head = 4 * xg + w, g = l >> 4, n = l & 15;
  char* kvh = ws + WS_KV + (size_t)head * KV_HEAD;
  char* qlo = ws + WS_QLO + (size_t)head * 65536;
  unsigned* qhi = (unsigned*)(ws + WS_QHI) + head * 2048;
#pragma unroll
  for (int mb = 0; mb < 4; ++mb) {
    const int tok0 = r * 64 + mb * 16, t = tok0 >> 5, s = mb & 1, key = 16 * s + n;
    const int off = t * 1024 + (g >> 1) * 512 + key * 16 + (g & 1) * 8;
    *(u32x2*)(qlo + off) = u32x2{pk2(aq[mb][0] * QSCALE, aq[mb][1] * QSCALE), pk2(aq[mb][2] * QSCALE, aq[mb][3] * QSCALE)};
    *(u32x2*)(kvh + off) = u32x2{pk2(ak[mb][0], ak[mb][1]), pk2(ak[mb][2], ak[mb][3])};
    char* vt = kvh + KV_VT + t * VT_TILE + s * 608;
    *(u32x2*)(vt + (g & 1) * 304 + n * 16 + (g >> 1) * 8) = u32x2{pk2(av[mb][0], av[mb][1]), pk2(av[mb][2], av[mb][3])};
    const int voff = ((n >> 2) & 1) * 304 + (4 * (n >> 3) + (n & 3)) * 2;
    if (g == 0) {
      qhi[tok0 + n] = pk2(am[mb][0] * QSCALE, am[mb][1] * QSCALE);
      *(u32x2*)(kvh + KV_KHI + (tok0 + n) * 8) = u32x2{pk2(am[mb][2], am[mb][3]), 0x00003f80u};
    } else if (g == 1) {
      const unsigned v = pk2(am[mb][0], am[mb][1]);
      *(u16*)(vt + voff + 16 * 16) = (u16)v;
      *(u16*)(vt + voff + 17 * 16) = (u16)(v >> 16);
    } else if (g == 2) {
      *(u16*)(vt + voff + 18 * 16) = (u16)0x3f80;
    }
  }
}

DI Unit retile_unit(int i, int xg, const u16* __restrict__ wo, const u16* __restrict__ w1, const u16* __restrict__ w2, char* __restrict__ ws) {
  Unit u;
  if (i < 5184) {
    const int kg = i / 72, cg = i % 72, wv = cg / 18, cl = cg % 18, ks = kg >> 2, kq = kg & 3;
    u.src = w1 + (size_t)kg * 8 * F_ + xg * 576 + cg * 8; u.ld = F_; u.a = 4; u.b = 2;
    u.pa = ws + WS_W12 + (size_t)(((xg * 4 + wv) * 36 + ks) * 9 + (cl >> 1)) * 1024 + (kq * 16 + (cl & 1) * 8) * 16;
    u.pb = u.pa + 64;
  } else if (i < 10368) {
    const int i2 = i - 5184, kg = i2 / 72, cg = i2 % 72, wv = cg / 18, cl = cg % 18, ks = kg >> 2, kq = kg & 3;
    const int row = ks < 16 ? (ks >> 2) * 144 + (ks & 3) * 32 + 4 * kq : 2 * (ks - 16) * 144 + 128 + 4 * kq;
    u.src = w2 + (size_t)(xg * 576 + row) * D_ + cg * 8; u.ld = D_; u.a = ks < 16 ? 16 : 144; u.b = 2;
    char* f = ws + WS_W12 + (size_t)(((xg * 4 + wv) * 36 + 18 + ks) * 9) * 1024 + kq * 256;
    u.pa = cl < 16 ? f + (cl >> 2) * 2048 + (cl & 3) * 64 : f + 8192 + (cl - 16) * 128;
    u.pb = u.pa + (cl < 16 ? 1024 : 64);
  } else {
    const int i3 = xg * 648 + (i - 10368), kg = i3 / 72, cg = i3 % 72, ks = kg >> 2, kq = kg & 3;
    const int xx = kg / 9, uu = kg % 9;
    const int row = uu < 8 ? (4 * xx + (uu >> 1)) * 18 + 8 * (uu & 1) : 4 * xx * 18 + 16;
    u.src = wo + (size_t)row * D_ + cg * 8; u.ld = D_; u.a = uu < 8 ? 4 : 36; u.b = uu < 8 ? 2 : 18;
    u.pa = ws + WS_WO + (size_t)((cg >> 1) * 18 + ks) * 1024 + (kq * 16 + (cg & 1) * 8) * 16;
    u.pb = u.pa + 64;
  }
  return u;
}

__global__ __launch_bounds__(768) void k_attn(const u16* __restrict__ wo, const u16* __restrict__ w1, const u16* __restrict__ w2,
                                              char* __restrict__ ws) {
  extern __shared__ __attribute__((aligned(16))) char lds[];
  const int b = blockIdx.x, tid = threadIdx.x, l = tid & 63;
  const int w = __builtin_amdgcn_readfirstlane(tid >> 6);
  const int xg = b & 7;
  if (w >= 8) {
    const int i0 = ((b >> 3) * 4 + (w - 8)) * 64 + l, i1 = i0 + 8192;
    u32x4 in0[8], in1[8];
    const Unit u0 = retile_unit(i0, xg, wo, w1, w2, ws);
    unit_load(u0, in0);
    const bool two = i1 < 11016;
    const Unit u1 = retile_unit(two ? i1 : i0, xg, wo, w1, w2, ws);
    if (two) unit_load(u1, in1);
    unit_store(u0, in0);
    if (two) unit_store(u1, in1);
    return;
  }
  const int hl = (b >> 3) & 3, head = 4 * xg + hl, qb = b >> 5;
  const int r32 = l & 31, h = l >> 5;
  const char* kvh = ws + WS_KV + (size_t)head * KV_HEAD;
  const int tq = qb * 8 + w;
  u32x4 qlo; unsigned qh0;
  {
    const char* pq = ws + WS_QLO + (size_t)head * 65536 + tq * 1024 + l * 16;
    const char* ph = ws + WS_QHI + (size_t)head * 8192 + (tq * 32 + r32) * 4;
    asm volatile("global_load_dwordx4 %0, %1, off" : "=v"(qlo) : "v"(pq) : "memory");
    asm volatile("global_load_dword %0, %1, off" : "=v"(qh0) : "v"(ph) : "memory");
  }
  const unsigned lds0 = (unsigned)(uintptr_t)lds;
#pragma unroll
  for (int G = 0; G < 4; ++G)
#pragma unroll
    for (int m = 0; m < 5; ++m) {
      const int i = min(w + 8 * m, 38);
      const int off = i < 16 ? (G * 16 + i) * 1024 : (i < 20 ? KV_KHI + G * 4096 + (i - 16) * 1024 : KV_VT + G * 19456 + (i - 20) * 1024);
      glds16(kvh + off + l * 16, (unsigned)__builtin_amdgcn_readfirstlane(lds0 + off));
    }
  asm volatile("s_waitcnt vmcnt(15)\n\ts_barrier" : "+v"(qlo), "+v"(qh0) :: "memory");
  const char* pk_lo = lds + l * 16;
  const char* pk_hi = lds + KV_KHI + r32 * 8;
  const char* pv = lds + KV_VT + h * 304 + min(r32, 18) * 16;
  u32x2 qhi = u32x2{h == 0 ? qh0 : 0u, 0u};
  const f32x16 zero16 = {0, 0, 0, 0, 0, 0, 0, 0, 0, 0, 0, 0, 0, 0, 0, 0};
  {
    f32x16 s0 = mfma32(*(const u32x4*)pk_lo, qlo, zero16);
    s0 = mfma32k8(*(const u32x2*)pk_hi, qhi, s0);
    float m = s0[0];
#pragma unroll
    for (int i = 1; i < 16; ++i) m = fmaxf(m, s0[i]);
    auto rr = __builtin_amdgcn_permlane32_swap(__float_as_uint(m), __float_as_uint(m), false, false);
    m = fmaxf(__uint_as_float(rr[0]), __uint_as_float(rr[1]));
    qhi[1] = h == 0 ? (pk2(-m, 0.f) & 0xffffu) : 0u;
  }
  f32x16 o = zero16;
#pragma unroll
  for (int G = 0; G < 4; ++G) {
    if (G == 1) asm volatile("s_waitcnt vmcnt(10)\n\ts_barrier" ::: "memory");
    if (G == 2) asm volatile("s_waitcnt vmcnt(5)\n\ts_barrier" ::: "memory");
    if (G == 3) asm volatile("s_waitcnt vmcnt(0)\n\ts_barrier" ::: "memory");
#pragma unroll 2
    for (int tt = 0; tt < 16; ++tt) {
      const int t = G * 16 + tt;
      const u32x4 kl = *(const u32x4*)(pk_lo + t * 1024);
      const u32x2 kh = *(const u32x2*)(pk_hi + t * 256);
      const u32x4 v0 = *(const u32x4*)(pv + t * VT_TILE);
      const u32x4 v1 = *(const u32x4*)(pv + t * VT_TILE + 608);
      f32x16 s = mfma32(kl, qlo, zero16);
      s = mfma32k8(kh, qhi, s);
      u32x4 p0, p1;
#pragma unroll
      for (int j = 0; j < 4; ++j) {
        p0[j] = pk2(fast_exp2(s[2 * j]), fast_exp2(s[2 * j + 1]));
        p1[j] = pk2(fast_exp2(s[8 + 2 * j]), fast_exp2(s[8 + 2 * j + 1]));
      }
      o = mfma32(v0, p0, o);
      o = mfma32(v1, p1, o);
    }
  }
  {
    bool bad = false;
#pragma unroll
    for (int i = 0; i < 11; ++i) bad |= !(fabsf(o[i]) < 3.0e38f);
    if (__any(bad)) {
      qhi[1] = 0u;
      float m = -3.0e38f;
      o = zero16;
      for (int t = 0; t < 64; ++t) {
        const u32x4 kl = *(const u32x4*)(pk_lo + t * 1024);
        const u32x2 kh = *(const u32x2*)(pk_hi + t * 256);
        const u32x4 v0 = *(const u32x4*)(pv + t * VT_TILE);
        const u32x4 v1 = *(const u32x4*)(pv + t * VT_TILE + 608);
        f32x16 s = mfma32(kl, qlo, zero16);
        s = mfma32k8(kh, qhi, s);
        float tm = s[0];
#pragma unroll
        for (int i = 1; i < 16; ++i) tm = fmaxf(tm, s[i]);
        auto rr = __builtin_amdgcn_permlane32_swap(__float_as_uint(tm), __float_as_uint(tm), false, false);
        tm = fmaxf(__uint_as_float(rr[0]), __uint_as_float(rr[1]));
        const float mn = fmaxf(m, tm), al = fast_exp2(m - mn);
        m = mn;
#pragma unroll
        for (int i = 0; i < 16; ++i) o[i] *= al;
        u32x4 p0, p1;
#pragma unroll
        for (int j = 0; j < 4; ++j) {
          p0[j] = pk2(fast_exp2(s[2 * j] - m), fast_exp2(s[2 * j + 1] - m));
          p1[j] = pk2(fast_exp2(s[8 + 2 * j] - m), fast_exp2(s[8 + 2 * j + 1] - m));
        }
        o = mfma32(v0, p0, o);
        o = mfma32(v1, p1, o);
      }
    }
  }
  const float inv = 1.f / o[10];
  const unsigned a0 = pk2(o[0] * inv, o[1] * inv), a1 = pk2(o[2] * inv, o[3] * inv);
  const unsigned b0 = pk2(o[4] * inv, o[5] * inv), b1 = pk2(o[6] * inv, o[7] * inv);
  const unsigned tail = pk2(o[8] * inv, o[9] * inv);
  auto r0 = __builtin_amdgcn_permlane32_swap(a0, b0, false, false);
  auto r1 = __builtin_amdgcn_permlane32_swap(a1, b1, false, false);
  const int tok = qb * 256 + w * 32 + r32, mb = tok >> 4, row = tok & 15;
  char* ot = ws + WS_OT + (size_t)mb * 18432 + row * 16;
  const int bi = xg * 9 + 2 * hl + h, bt = xg * 9 + 8;
  *(u32x4*)(ot + (bi >> 2) * 1024 + (bi & 3) * 256) = u32x4{r0[0], r1[0], r0[1], r1[1]};
  if (h == 0) *(unsigned*)(ot + (bt >> 2) * 1024 + (bt & 3) * 256 + hl * 4) = tail;
}

__global__ __launch_bounds__(192) void k_wo(const u16* __restrict__ x, char* __restrict__ ws) {
  __shared__ __attribute__((aligned(16))) char A[36864];
  const int b = blockIdx.x, tid = threadIdx.x, l = tid & 63;
  const int w = __builtin_amdgcn_readfirstlane(tid >> 6);
  const int cb = b & 3, rb = b >> 2;
  const char* src = ws + WS_OT + (size_t)rb * 36864;
  u32x4 av[12];
#pragma unroll
  for (int i = 0; i < 12; ++i) av[i] = *(const u32x4*)(src + (w * 12 + i) * 1024 + l * 16);
  const char* wt = ws + WS_WO + (size_t)((cb * 9 + 3 * w) * 18) * 1024 + l * 16;
  u32x4 wf[18][3];
#pragma unroll
  for (int ks = 0; ks < 18; ++ks)
#pragma unroll
    for (int nb = 0; nb < 3; ++nb) wf[ks][nb] = *(const u32x4*)(wt + (nb * 18 + ks) * 1024);
#pragma unroll
  for (int i = 0; i < 12; ++i) *(u32x4*)(A + (w * 12 + i) * 1024 + l * 16) = av[i];
  __syncthreads();
  f32x4 acc[2][3];
#pragma unroll
  for (int mb = 0; mb < 2; ++mb)
#pragma unroll
    for (int nb = 0; nb < 3; ++nb) acc[mb][nb] = f32x4{0, 0, 0, 0};
#pragma unroll
  for (int ks = 0; ks < 18; ++ks)
#pragma unroll
    for (int mb = 0; mb < 2; ++mb) {
      const u32x4 af = *(const u32x4*)(A + (mb * 18 + ks) * 1024 + l * 16);
#pragma unroll
      for (int nb = 0; nb < 3; ++nb) acc[mb][nb] = mfma16(wf[ks][nb], af, acc[mb][nb]);
    }
  const float* ga = (const float*)(ws + WS_MOD) + 2 * 576;
  u16* x1 = (u16*)(ws + WS_X1);
  const int g = l >> 4, n = l & 15;
#pragma unroll
  for (int mb = 0; mb < 2; ++mb)
#pragma unroll
    for (int nb = 0; nb < 3; ++nb) {
      const int tok = rb * 32 + mb * 16 + n, col = (cb * 9 + 3 * w + nb) * 16 + 4 * g;
      const u32x2 xv = *(const u32x2*)(x + (size_t)tok * D_ + col);
      const f32x4 gv = *(const f32x4*)(ga + col);
      const f32x4 a = acc[mb][nb];
      *(u32x2*)(x1 + (size_t)tok * D_ + col) =
          u32x2{pk2(lo2f(xv[0]) + gv[0] * a[0], hi2f(xv[0]) + gv[1] * a[1]), pk2(lo2f(xv[1]) + gv[2] * a[2], hi2f(xv[1]) + gv[3] * a[3])};
    }
}

template <int NS, int NPRE>
DI void gemm_64x144(const char* __restrict__ wt, const char* A, int l, u32x4 (&wf)[NS][9], f32x4 (&acc)[4][9]) {
#pragma unroll
  for (int mb = 0; mb < 4; ++mb)
#pragma unroll
    for (int nb = 0; nb < 9; ++nb) acc[mb][nb] = f32x4{0, 0, 0, 0};
  u32x4 hf[2][4];
#pragma unroll
  for (int mb = 0; mb < 4; ++mb) hf[0][mb] = *(const u32x4*)(A + mb * 18432 + l * 16);
  static_assert(NS == 2, "");
  auto step = [&](int ks, auto kk_, bool pre) {
    constexpr int kk = decltype(kk_)::value;
#pragma unroll
    for (int nb = 0; nb < 9; ++nb) {
      if (nb < 4) hf[kk ^ 1][nb] = *(const u32x4*)(A + (nb * 18 + min(ks + 1, 17)) * 1024 + l * 16);
#pragma unroll
      for (int mb = 0; mb < 4; ++mb) acc[mb][nb] = mfma16(wf[kk][nb], hf[kk][mb], acc[mb][nb]);
      if (pre) wf[kk][nb] = *(const u32x4*)(wt + ((ks + NS) * 9 + nb) * 1024);
      SB();
    }
  };
#pragma unroll
  for (int ks = 0; ks < NPRE; ks += 2) {
    step(ks, std::integral_constant<int, 0>{}, true);
    step(ks + 1, std::integral_constant<int, 1>{}, true);
  }
#pragma unroll
  for (int ks = NPRE; ks < 18; ks += 2) {
    step(ks, std::integral_constant<int, 0>{}, false);
    step(ks + 1, std::integral_constant<int, 1>{}, false);
  }
}

__global__ __launch_bounds__(256, 1) void k_ffn(char* __restrict__ ws) {
  __shared__ __attribute__((aligned(16))) char A[73728];
  __shared__ __attribute__((aligned(16))) float gs[1152];
  const int b = blockIdx.x, tid = threadIdx.x, l = tid & 63;
  const int w = __builtin_amdgcn_readfirstlane(tid >> 6);
  const int xg = b & 7, r = b >> 3;
  const char* wt = ws + WS_W12 + (size_t)((xg * 4 + w) * 324) * 1024 + l * 16;
  constexpr int NS = 2;
  u32x4 wf[NS][9];
#pragma unroll
  for (int ks = 0; ks < NS; ++ks)
#pragma unroll
    for (int nb = 0; nb < 9; ++nb) wf[ks][nb] = *(const u32x4*)(wt + (ks * 9 + nb) * 1024);
  SB();
  {
    const float* mod = (const float*)(ws + WS_MOD);
    for (int i = tid; i < 576; i += 256) { gs[i] = mod[4 * 576 + i]; gs[576 + i] = mod[3 * 576 + i]; }
  }
  ln_tile<true>((const u16*)(ws + WS_X1) + (size_t)r * 64 * D_, nullptr, gs, A, w, l);
  __syncthreads();
  f32x4 acc[4][9];
  gemm_64x144<NS, 18>(wt, A, l, wf, acc);
  u32x2 xp[4][9];
#pragma unroll
  for (int mb = 0; mb < 4; ++mb)
#pragma unroll
    for (int nb = 0; nb < 9; ++nb)
      xp[mb][nb] = u32x2{pk2(silu_f(acc[mb][nb][0]), silu_f(acc[mb][nb][1])), pk2(silu_f(acc[mb][nb][2]), silu_f(acc[mb][nb][3]))};
  __syncthreads();
#pragma unroll
  for (int mb = 0; mb < 4; ++mb) {
#pragma unroll
    for (int p = 0; p < 4; ++p)
      *(u32x4*)(A + (mb * 18 + w * 4 + p) * 1024 + l * 16) = u32x4{xp[mb][2 * p][0], xp[mb][2 * p][1], xp[mb][2 * p + 1][0], xp[mb][2 * p + 1][1]};
    *(u32x2*)(A + (mb * 18 + 16 + (w >> 1)) * 1024 + l * 16 + (w & 1) * 8) = xp[mb][8];
  }
  __syncthreads();
  gemm_64x144<NS, 16>(wt + 162 * 1024, A, l, wf, acc);
  char* part = ws + WS_PART + (size_t)(xg * 32 + r) * 73728 + w * 18432;
#pragma unroll
  for (int mb = 0; mb < 4; ++mb) {
#pragma unroll
    for (int p = 0; p < 4; ++p)
      *(u32x4*)(part + mb * 4608 + p * 1024 + l * 16) =
          u32x4{pk2(acc[mb][2 * p][0], acc[mb][2 * p][1]), pk2(acc[mb][2 * p][2], acc[mb][2 * p][3]),
                pk2(acc[mb][2 * p + 1][0], acc[mb][2 * p + 1][1]), pk2(acc[mb][2 * p + 1][2], acc[mb][2 * p + 1][3])};
    *(u32x2*)(part + mb * 4608 + 4096 + l * 8) = u32x2{pk2(acc[mb][8][0], acc[mb][8][1]), pk2(acc[mb][8][2], acc[mb][8][3])};
  }
}

__global__ __launch_bounds__(256) void k_red(const char* __restrict__ ws, u16* __restrict__ out) {
  const int b = blockIdx.x, tid = threadIdx.x, l = tid & 63;
  const int v = __builtin_amdgcn_readfirstlane(tid >> 6);
  const int r = b >> 3, wm = 2 * (b & 7) + (v >> 1), wv = wm >> 2, mb = wm & 3;
  const char* part = ws + WS_PART + (size_t)r * 73728 + wm * 4608;
  const u16* x1 = (const u16*)(ws + WS_X1);
  const float* gb = (const float*)(ws + WS_MOD) + 5 * 576;
  const int g = l >> 4, tok = r * 64 + mb * 16 + (l & 15);
  u32x4 pv[2][8]; u32x2 sv[8];
#pragma unroll
  for (int i = 0; i < 2; ++i)
#pragma unroll
    for (int xg = 0; xg < 8; ++xg) pv[i][xg] = *(const u32x4*)(part + (size_t)xg * 32 * 73728 + (2 * (v & 1) + i) * 1024 + l * 16);
  if ((v & 1) == 0) {
#pragma unroll
    for (int xg = 0; xg < 8; ++xg) sv[xg] = *(const u32x2*)(part + (size_t)xg * 32 * 73728 + 4096 + l * 8);
  }
#pragma unroll
  for (int i = 0; i < 2; ++i) {
    const int col = wv * 144 + 32 * (2 * (v & 1) + i) + 8 * g;
    const u32x4 xv = *(const u32x4*)(x1 + (size_t)tok * D_ + col);
    const f32x4 g0 = *(const f32x4*)(gb + col), g1 = *(const f32x4*)(gb + col + 4);
    u32x4 o;
#pragma unroll
    for (int j = 0; j < 4; ++j) {
      float a = 0.f, c = 0.f;
#pragma unroll
      for (int xg = 0; xg < 8; ++xg) { a += lo2f(pv[i][xg][j]); c += hi2f(pv[i][xg][j]); }
      const float ga = j < 2 ? g0[2 * j] : g1[2 * j - 4], gc = j < 2 ? g0[2 * j + 1] : g1[2 * j - 3];
      o[j] = pk2(lo2f(xv[j]) + ga * a, hi2f(xv[j]) + gc * c);
    }
    *(u32x4*)(out + (size_t)tok * D_ + col) = o;
  }
  if ((v & 1) == 0) {
    const int col = wv * 144 + 128 + 4 * g;
    const u32x2 xv = *(const u32x2*)(x1 + (size_t)tok * D_ + col);
    const f32x4 g0 = *(const f32x4*)(gb + col);
    u32x2 o;
#pragma unroll
    for (int j = 0; j < 2; ++j) {
      float a = 0.f, c = 0.f;
#pragma unroll
      for (int xg = 0; xg < 8; ++xg) { a += lo2f(sv[xg][j]); c += hi2f(sv[xg][j]); }
      o[j] = pk2(lo2f(xv[j]) + g0[2 * j] * a, hi2f(xv[j]) + g0[2 * j + 1] * c);
    }
    *(u32x2*)(out + (size_t)tok * D_ + col) = o;
  }
}

extern "C" void kernel_launch(void* const* d_in, const int* in_sizes, int n_in, void* d_out, int out_size, void* d_ws, size_t ws_size,
                              hipStream_t stream) {
  const u16* x = (const u16*)d_in[0];
  const u16* wqkv = (const u16*)d_in[1];
  const u16* wo = (const u16*)d_in[2];
  const u16* w1 = (const u16*)d_in[4];
  const u16* w2 = (const u16*)d_in[5];
  const u16* c = (const u16*)d_in[6];
  const u16* wmod = (const u16*)d_in[7];
  char* ws = (char*)d_ws;
  (void)hipFuncSetAttribute((const void*)k_attn, hipFuncAttributeMaxDynamicSharedMemorySize, KV_HEAD);
  k_prep<<<256, 448, 0, stream>>>(x, wqkv, c, wmod, ws);
  k_qkv<<<256, 256, 0, stream>>>(x, ws);
  k_attn<<<256, 768, KV_HEAD, stream>>>(wo, w1, w2, ws);
  k_wo<<<256, 192, 0, stream>>>(x, ws);
  k_ffn<<<256, 256, 0, stream>>>(ws);
  k_red<<<256, 256, 0, stream>>>(ws, (u16*)d_out);
}
```

```cpp
#include <hip/hip_runtime.h>
#include <hip/hip_bf16.h>
#include <stdint.h>
#include <type_traits>

typedef __attribute__((ext_vector_type(8))) __bf16 bf16x8;
typedef __attribute__((ext_vector_type(2))) __bf16 bf16x2;
typedef __attribute__((ext_vector_type(4))) short s16x4;
typedef __attribute__((ext_vector_type(2))) float f32x2;
typedef __attribute__((ext_vector_type(4))) float f32x4;
typedef __attribute__((ext_vector_type(16))) float f32x16;
typedef __attribute__((ext_vector_type(2))) unsigned u32x2;
typedef __attribute__((ext_vector_type(4))) unsigned u32x4;
typedef unsigned short u16;
typedef __attribute__((address_space(1))) unsigned gu32;

#define DI __device__ __forceinline__
#define SB() __builtin_amdgcn_sched_barrier(0)

constexpr int S_ = 2048, D_ = 576, F_ = 4608;
constexpr float LN_EPS = 1e-5f;
constexpr float QSCALE = 0.23570226039551584f * 1.4426950408889634f;

constexpr size_t WS_FLAG = 0;
constexpr size_t WS_MOD  = 20480;
constexpr size_t WS_STAT = 36864;
constexpr size_t WS_WQKV = 53248;
constexpr size_t WS_QLO  = WS_WQKV + 2064384;
constexpr size_t WS_QHI  = WS_QLO + 2097152;
constexpr int    KV_HEAD = 159744, KV_KHI = 65536, KV_VT = 81920, VT_TILE = 1216;
constexpr size_t WS_KV   = WS_QHI + 262144;
constexpr size_t WS_OT   = WS_KV + 32 * (size_t)KV_HEAD;
constexpr size_t WS_WO   = WS_OT + 2359296;
constexpr size_t WS_W12  = WS_WO + 663552;
constexpr size_t WS_X1   = WS_W12 + 2 * 5308416;
constexpr size_t WS_PART = WS_X1 + 2359296;
constexpr size_t WS_END  = WS_PART + 18874368;

#define TS(K, I)

DI float bf2f(u16 v) { return __uint_as_float((unsigned)v << 16); }
DI float lo2f(unsigned v) { return __uint_as_float(v << 16); }
DI float hi2f(unsigned v) { return __uint_as_float(v & 0xffff0000u); }
DI unsigned pk2(float a, float b) { bf16x2 v = {(__bf16)a, (__bf16)b}; return __builtin_bit_cast(unsigned, v); }
DI float rbf(float a) { return (float)(__bf16)a; }
DI f32x4 mfma16(u32x4 a, u32x4 b, f32x4 c) {
  return __builtin_amdgcn_mfma_f32_16x16x32_bf16(__builtin_bit_cast(bf16x8, a), __builtin_bit_cast(bf16x8, b), c, 0, 0, 0);
}
DI f32x16 mfma32(u32x4 a, u32x4 b, f32x16 c) {
  return __builtin_amdgcn_mfma_f32_32x32x16_bf16(__builtin_bit_cast(bf16x8, a), __builtin_bit_cast(bf16x8, b), c, 0, 0, 0);
}
DI f32x16 mfma32k8(u32x2 a, u32x2 b, f32x16 c) {
  return __builtin_amdgcn_mfma_f32_32x32x8bf16_1k(__builtin_bit_cast(s16x4, a), __builtin_bit_cast(s16x4, b), c, 0, 0, 0);
}
DI float fast_rcp(float x) { return __builtin_amdgcn_rcpf(x); }
DI float fast_exp2(float x) { return __builtin_amdgcn_exp2f(x); }
DI float silu_f(float x) { return x * fast_rcp(1.f + fast_exp2(-1.4426950408889634f * x)); }
DI float xor_add(float v, int m) { return v + __shfl_xor(v, m); }
DI float dot2(unsigned a, unsigned b, float c) {
  return __builtin_amdgcn_fdot2_f32_bf16(__builtin_bit_cast(bf16x2, a), __builtin_bit_cast(bf16x2, b), c, false);
}
DI float red_sub(float v) {
  v += __builtin_bit_cast(float, __builtin_amdgcn_update_dpp(0, __builtin_bit_cast(int, v), 0x128, 0xf, 0xf, false));
  auto a = __builtin_amdgcn_permlane16_swap(__float_as_uint(v), __float_as_uint(v), false, false);
  v = __uint_as_float(a[0]) + __uint_as_float(a[1]);
  auto b = __builtin_amdgcn_permlane32_swap(__float_as_uint(v), __float_as_uint(v), false, false);
  return __uint_as_float(b[0]) + __uint_as_float(b[1]);
}
DI u32x4 join16(u32x2 x, u32x2 y) {
  auto a = __builtin_amdgcn_permlane16_swap(x[0], y[0], false, false);
  auto b = __builtin_amdgcn_permlane16_swap(x[1], y[1], false, false);
  return u32x4{a[0], b[0], a[1], b[1]};
}
DI u32x4 join32(u32x2 x, u32x2 y) {
  auto a = __builtin_amdgcn_permlane32_swap(x[0], y[0], false, false);
  auto b = __builtin_amdgcn_permlane32_swap(x[1], y[1], false, false);
  return u32x4{a[0], b[0], a[1], b[1]};
}
DI void glds16(const void* g, unsigned lds_base) {
  unsigned sv;
  asm volatile("s_mov_b32 %0, m0\n\ts_mov_b32 m0, %2\n\ts_nop 0\n\tglobal_load_lds_dwordx4 %1, off\n\ts_mov_b32 m0, %0"
               : "=&s"(sv) : "v"(g), "s"(lds_base) : "memory");
}
DI __amdgpu_buffer_rsrc_t ws_rsrc(char* ws) { return __builtin_amdgcn_make_buffer_rsrc(ws, 0, (int)WS_END, 0x00020000); }
DI void st_dev(__amdgpu_buffer_rsrc_t rs, unsigned off, u32x4 v) { __builtin_amdgcn_raw_buffer_store_b128(v, rs, off, 0, 16); }
DI u32x4 ld_dev(__amdgpu_buffer_rsrc_t rs, unsigned off) { return __builtin_amdgcn_raw_buffer_load_b128(rs, off, 0, 16); }
DI void publish(gu32* cnt) {
  asm volatile("s_waitcnt vmcnt(0)" ::: "memory");
  __syncthreads();
  if (threadIdx.x == 0) __hip_atomic_fetch_add(cnt, 1u, __ATOMIC_RELAXED, __HIP_MEMORY_SCOPE_AGENT);
}
DI void await(gu32* cnt, unsigned want, gu32* tmo) {
  if (threadIdx.x == 0) {
    for (unsigned spins = 0; __hip_atomic_load(cnt, __ATOMIC_RELAXED, __HIP_MEMORY_SCOPE_AGENT) < want;) {
      __builtin_amdgcn_s_sleep(1);
      if (++spins > (1u << 20)) { __hip_atomic_store(tmo, 1u, __ATOMIC_RELAXED, __HIP_MEMORY_SCOPE_AGENT); break; }
    }
  }
  __builtin_amdgcn_fence(__ATOMIC_ACQUIRE, "wavefront");
  __syncthreads();
}

struct Unit { const u16* src; char* p; int ld, a, b, c, s1, s2; };
DI void unit_load(const Unit& u, u32x4 (&in)[8]) {
#pragma unroll
  for (int j = 0; j < 8; ++j) in[j] = *(const u32x4*)(u.src + (size_t)(u.a * (j >> 2) + u.b * ((j >> 1) & 1) + u.c * (j & 1)) * u.ld);
}
DI u32x4 unit_col(const u32x4 (&in)[8], int i) {
  u32x4 o;
#pragma unroll
  for (int m = 0; m < 4; ++m) o[m] = __builtin_amdgcn_perm(in[2 * m + 1][i >> 1], in[2 * m][i >> 1], (i & 1) ? 0x07060302u : 0x05040100u);
  return o;
}

__global__ __launch_bounds__(448) void k_prep(const u16* __restrict__ x, const u16* __restrict__ wqkv, const u16* __restrict__ c,
                                              const u16* __restrict__ wmod, char* __restrict__ ws) {
  __shared__ float red[128 * 16];
  const int b = blockIdx.x, tid = threadIdx.x, l = tid & 63;
  const int w = __builtin_amdgcn_readfirstlane(tid >> 6);
  TS(0, 0);
  if (w < 4) {
    if (b >= 216) {
      if (b == 255) for (int i = tid; i < 5120; i += 256) ((unsigned*)(ws + WS_FLAG))[i] = 0u;
      return;
    }
    const int cg = (b & 7) * 27 + (b >> 3);
    const int half = tid & 1, row0 = tid >> 1;
    u32x4 wv[5]; u16 cv[5];
#pragma unroll
    for (int i = 0; i < 5; ++i) {
      const int k = min(row0 + 128 * i, 575);
      wv[i] = *(const u32x4*)(wmod + (size_t)k * 3456 + cg * 16 + half * 8);
      cv[i] = c[k];
    }
    float acc[8];
#pragma unroll
    for (int j = 0; j < 8; ++j) acc[j] = 0.f;
#pragma unroll
    for (int i = 0; i < 5; ++i) {
      float s = rbf(silu_f(bf2f(cv[i])));
      if (row0 + 128 * i >= 576) s = 0.f;
#pragma unroll
      for (int j = 0; j < 4; ++j) { acc[2 * j] += s * lo2f(wv[i][j]); acc[2 * j + 1] += s * hi2f(wv[i][j]); }
    }
    *(f32x4*)&red[row0 * 16 + half * 8] = f32x4{acc[0], acc[1], acc[2], acc[3]};
    *(f32x4*)&red[row0 * 16 + half * 8 + 4] = f32x4{acc[4], acc[5], acc[6], acc[7]};
    __syncthreads();
    if (w == 0) {
      const int col = l & 15, part = l >> 4;
      float s = 0.f;
#pragma unroll
      for (int i = 0; i < 32; ++i) s += red[(part * 32 + i) * 16 + col];
      s = xor_add(s, 16); s = xor_add(s, 32);
      if (l < 16) {
        const int n = cg * 16 + col, sec = n / 576;
        float v = rbf(s);
        if (sec == 1 || sec == 4) v = rbf(1.f + v);
        ((float*)(ws + WS_MOD))[n] = v;
      }
    }
    TS(0, 1);
  } else if (w == 4) {
    const int row = 8 * b + (l & 7), sub = l >> 3;
    u32x4 xv[9];
#pragma unroll
    for (int i = 0; i < 9; ++i) xv[i] = *(const u32x4*)(x + (size_t)row * D_ + (sub + 8 * i) * 8);
    float s = 0.f;
#pragma unroll
    for (int i = 0; i < 9; ++i)
#pragma unroll
      for (int j = 0; j < 4; ++j) s += lo2f(xv[i][j]) + hi2f(xv[i][j]);
    const float mu = red_sub(s) * (1.f / D_);
    float q = 0.f;
#pragma unroll
    for (int i = 0; i < 9; ++i)
#pragma unroll
      for (int j = 0; j < 4; ++j) { const float a = lo2f(xv[i][j]) - mu, bb = hi2f(xv[i][j]) - mu; q += a * a + bb * bb; }
    q = red_sub(q);
    const float rstd = 1.f / sqrtf(q * (1.f / D_) + LN_EPS);
    if (sub == 0) ((f32x2*)(ws + WS_STAT))[row] = f32x2{rstd, -mu * rstd};
    TS(0, 1);
  } else {
    const int xg = b & 7, rank = b >> 3;
    char* dst = ws + WS_WQKV + (size_t)xg * 252 * 1024;
    if (w == 5) {
      if (l < 54) {
        const int i = rank * 54 + l, kg = i / 24, rem = i % 24, part = rem >> 3, hl = (rem >> 1) & 3, half = rem & 1;
        Unit u;
        u.src = wqkv + (size_t)kg * 8 * 1728 + part * 576 + (4 * xg + hl) * 18 + half * 8;
        u.ld = 1728; u.a = 4; u.b = 2; u.c = 1;
        char* p = dst + ((kg >> 2) * 14 + (part < 2 ? 2 * hl + part : 8 + hl)) * 1024 + ((kg & 3) * 16 + half * 8) * 16;
        u32x4 in[8];
        unit_load(u, in);
#pragma unroll
        for (int i2 = 0; i2 < 8; ++i2) *(u32x4*)(p + i2 * 16) = unit_col(in, i2);
      }
    } else {
      if (l < 9) {
        const int i = rank * 9 + l, kg = i >> 2, hl = i & 3;
        const u16* src = wqkv + (size_t)kg * 8 * 1728 + (4 * xg + hl) * 18 + 16;
        unsigned in[3][8];
#pragma unroll
        for (int p = 0; p < 3; ++p)
#pragma unroll
          for (int j = 0; j < 8; ++j) in[p][j] = *(const unsigned*)(src + (size_t)j * 1728 + p * 576);
        char* pq = dst + ((kg >> 2) * 14 + 12) * 1024 + (kg & 3) * 256;
#pragma unroll
        for (int p = 0; p < 3; ++p)
#pragma unroll
          for (int e = 0; e < 2; ++e) {
            u32x4 o;
#pragma unroll
            for (int m = 0; m < 4; ++m) o[m] = __builtin_amdgcn_perm(in[p][2 * m + 1], in[p][2 * m], e ? 0x07060302u : 0x05040100u);
            if (p < 2) *(u32x4*)(pq + (4 * hl + 2 * p + e) * 16) = o;
            else { *(u32x4*)(pq + 1024 + (2 * hl + e) * 16) = o; *(u32x4*)(pq + 1024 + (8 + 2 * hl + e) * 16) = u32x4{0, 0, 0, 0}; }
          }
      }
    }
    TS(0, 1);
  }
}

DI void ln_apply(const u32x4 (&xv)[9], f32x2 st, const float* gs, char* A, int w, int l) {
  const int sub = l >> 3;
#pragma unroll
  for (int i = 0; i < 9; ++i) {
    const int ch = sub + 8 * i;
    const f32x4 g0 = *(const f32x4*)(gs + ch * 8), g1 = *(const f32x4*)(gs + ch * 8 + 4);
    const f32x4 s0 = *(const f32x4*)(gs + 576 + ch * 8), s1 = *(const f32x4*)(gs + 576 + ch * 8 + 4);
    u32x4 o;
#pragma unroll
    for (int j = 0; j < 4; ++j) {
      const float ga = j < 2 ? g0[2 * j] : g1[2 * j - 4], gb = j < 2 ? g0[2 * j + 1] : g1[2 * j - 3];
      const float sa = j < 2 ? s0[2 * j] : s1[2 * j - 4], sb = j < 2 ? s0[2 * j + 1] : s1[2 * j - 3];
      const float a = (lo2f(xv[i][j]) * st[0] + st[1]) * ga + sa;
      const float bb = (hi2f(xv[i][j]) * st[0] + st[1]) * gb + sb;
      o[j] = pk2(a, bb);
    }
    *(u32x4*)(A + (w >> 1) * 18432 + ch * 256 + ((w & 1) * 8 + (l & 7)) * 16) = o;
  }
}

template <bool TFORM>
DI void qkv_gemm(const char* __restrict__ wt, const char* A, int s, int l, f32x4 (&a0)[4], f32x4 (&a1)[4], f32x4& at,
                 u32x4 (&wf)[6][3]) {
  constexpr int NS = 6;
  u32x4 hf[2][4];
#pragma unroll
  for (int i = 0; i < 4; ++i) hf[0][i] = *(const u32x4*)(A + ((i + s) & 3) * 18432 + l * 16);
#pragma unroll
  for (int ks = 0; ks < 18; ++ks) {
    const int kk = ks % NS;
#pragma unroll
    for (int i = 0; i < 4; ++i) {
      if (ks + 1 < 18) hf[(ks + 1) & 1][i] = *(const u32x4*)(A + ((i + s) & 3) * 18432 + (ks + 1) * 1024 + l * 16);
      if (TFORM) {
        a0[i] = mfma16(wf[kk][0], hf[ks & 1][i], a0[i]);
        a1[i] = mfma16(wf[kk][1], hf[ks & 1][i], a1[i]);
        if (i == 0) at = mfma16(wf[kk][2], hf[ks & 1][0], at);
      } else {
        a0[i] = mfma16(hf[ks & 1][i], wf[kk][0], a0[i]);
        if (i == 0) at = mfma16(hf[ks & 1][0], wf[kk][2], at);
      }
      SB();
    }
    if (ks + NS < 18) {
      wf[kk][0] = *(const u32x4*)(wt + (ks + NS) * 14336);
      if (TFORM) wf[kk][1] = *(const u32x4*)(wt + (ks + NS) * 14336 + 1024);
      wf[kk][2] = *(const u32x4*)(wt + (ks + NS) * 14336 + (TFORM ? 12 - 2 * s : 13 - 8 - s) * 1024);
    }
    SB();
  }
}

__global__ __launch_bounds__(512, 2) void k_qkv(const u16* __restrict__ x, char* __restrict__ ws) {
  __shared__ __attribute__((aligned(16))) char A[73728];
  __shared__ __attribute__((aligned(16))) float gs[1152];
  const int b = blockIdx.x, tid = threadIdx.x, l = tid & 63;
  const int w = __builtin_amdgcn_readfirstlane(tid >> 6);
  const int xg = b & 7, r = b >> 3, s = w & 3;
  TS(1, 0);
  u32x4 xv[9];
  const int row = r * 64 + 8 * w + (l & 7);
#pragma unroll
  for (int i = 0; i < 9; ++i) xv[i] = *(const u32x4*)(x + (size_t)row * D_ + ((l >> 3) + 8 * i) * 8);
  const f32x2 st = ((const f32x2*)(ws + WS_STAT))[row];
  f32x4 gv = {0, 0, 0, 0};
  if (tid < 288) gv = *(const f32x4*)((const float*)(ws + WS_MOD) + (tid < 144 ? 576 + tid * 4 : tid * 4 - 576));
  const char* wt = ws + WS_WQKV + (size_t)xg * 252 * 1024 + (w < 4 ? 2 * s : 8 + s) * 1024 + l * 16;
  u32x4 wf[6][3];
#pragma unroll
  for (int ks = 0; ks < 6; ++ks) {
    wf[ks][0] = *(const u32x4*)(wt + ks * 14336);
    if (w < 4) wf[ks][1] = *(const u32x4*)(wt + ks * 14336 + 1024);
    wf[ks][2] = *(const u32x4*)(wt + ks * 14336 + (w < 4 ? 12 - 2 * s : 13 - 8 - s) * 1024);
  }
  SB();
  if (tid < 288) *(f32x4*)(gs + tid * 4) = gv;
  __syncthreads();
  ln_apply(xv, st, gs, A, w, l);
  TS(1, 1);
  __syncthreads();
  TS(1, 2);
  f32x4 a0[4], a1[4], at = {0, 0, 0, 0};
#pragma unroll
  for (int i = 0; i < 4; ++i) { a0[i] = at; a1[i] = at; }
  const int g = l >> 4, n = l & 15;
  const int head = 4 * xg + s;
  char* kvh = ws + WS_KV + (size_t)head * KV_HEAD;
  if (w < 4) {
    qkv_gemm<true>(wt, A, s, l, a0, a1, at, wf);
    TS(1, 3);
    char* qlo = ws + WS_QLO + (size_t)head * 65536;
#pragma unroll
    for (int i = 0; i < 4; i += 2) {
      const int mb = (i + (g & 1) + s) & 3;
      const int off = (2 * r + (mb >> 1)) * 1024 + (g >> 1) * 512 + (16 * (mb & 1) + n) * 16;
      u32x2 q0, q1, k0, k1;
      q0 = u32x2{pk2(a0[i][0] * QSCALE, a0[i][1] * QSCALE), pk2(a0[i][2] * QSCALE, a0[i][3] * QSCALE)};
      q1 = u32x2{pk2(a0[i + 1][0] * QSCALE, a0[i + 1][1] * QSCALE), pk2(a0[i + 1][2] * QSCALE, a0[i + 1][3] * QSCALE)};
      k0 = u32x2{pk2(a1[i][0], a1[i][1]), pk2(a1[i][2], a1[i][3])};
      k1 = u32x2{pk2(a1[i + 1][0], a1[i + 1][1]), pk2(a1[i + 1][2], a1[i + 1][3])};
      *(u32x4*)(qlo + off) = join16(q0, q1);
      *(u32x4*)(kvh + off) = join16(k0, k1);
    }
    const int tok = r * 64 + s * 16 + n, hg = 4 * xg + g;
    ((unsigned*)(ws + WS_QHI))[hg * 2048 + tok] = pk2(at[0] * QSCALE, at[1] * QSCALE);
    *(u32x2*)(ws + WS_KV + (size_t)hg * KV_HEAD + KV_KHI + tok * 8) = u32x2{pk2(at[2], at[3]), 0x00003f80u};
  } else {
    qkv_gemm<false>(wt, A, s, l, a0, a1, at, wf);
    TS(1, 3);
#pragma unroll
    for (int i = 0; i < 4; i += 2) {
      const int mb = (i + (g >> 1) + s) & 3;
      const u32x2 v0 = u32x2{pk2(a0[i][0], a0[i][1]), pk2(a0[i][2], a0[i][3])};
      const u32x2 v1 = u32x2{pk2(a0[i + 1][0], a0[i + 1][1]), pk2(a0[i + 1][2], a0[i + 1][3])};
      *(u32x4*)(kvh + KV_VT + (2 * r + (mb >> 1)) * VT_TILE + (mb & 1) * 608 + (g & 1) * 304 + n * 16) = join32(v0, v1);
    }
    if (n < 12) {
      const int hg = 4 * xg + (n < 8 ? n >> 1 : n - 8), vr = n < 8 ? 16 + (n & 1) : 18;
      const u32x2 v = n < 8 ? u32x2{pk2(at[0], at[1]), pk2(at[2], at[3])} : u32x2{0x3f803f80u, 0x3f803f80u};
      *(u32x2*)(ws + WS_KV + (size_t)hg * KV_HEAD + KV_VT + (2 * r + (s >> 1)) * VT_TILE + (s & 1) * 608 + (g & 1) * 304 + vr * 16 + (g >> 1) * 8) = v;
    }
  }
  TS(1, 4);
}

DI size_t w12_frag(int grp, int j, int step) {
  return grp < 4 ? grp * 180 + step * 5 + j : (grp < 8 ? 720 + (grp - 4) * 144 + step * 4 + j : j * 180 + step * 5 + 4);
}
DI Unit retile_unit(int u0, int xg, int l, const u16* __restrict__ wo, const u16* __restrict__ w1, const u16* __restrict__ w2, char* __restrict__ ws) {
  Unit u;
  const int cl = l & 7, kgl = l >> 3;
  char* w12 = ws + WS_W12 + (size_t)xg * 1296 * 1024;
  if (u0 < 81) {
    const int grp = u0 / 9, kg = (u0 % 9) * 8 + kgl, ks = kg >> 2, kq = kg & 3;
    u.src = w1 + (size_t)kg * 8 * F_ + xg * 576 + grp * 64 + cl * 8; u.ld = F_; u.a = 4; u.b = 2; u.c = 1;
    u.p = w12 + w12_frag(grp, 0, ks) * 1024 + kq * 256 + cl * 16;
    u.s1 = grp < 8 ? 1024 : 180 * 1024; u.s2 = 128;
  } else if (u0 < 162) {
    const int u2 = u0 - 81, grp = u2 / 9, kg = (u2 % 9) * 8 + kgl, ks = kg >> 2, kq = kg & 3;
    const int row = (ks >> 1) * 64 + 32 * (kq & 1) + 4 * (kq >> 1) + 2 * (ks & 1);
    const int step = 18 + (ks < 8 ? ks : (ks < 16 ? ks + 2 : ks - 8));
    u.src = w2 + (size_t)(xg * 576 + row) * D_ + grp * 64 + cl * 8; u.ld = D_; u.a = 1; u.b = 16; u.c = 8;
    if (grp < 8) { u.p = w12 + w12_frag(grp, 0, step) * 1024 + kq * 256 + cl * 16; u.s1 = 1024; u.s2 = 128; }
    else { u.p = w12 + w12_frag(8, cl >> 1, step) * 1024 + kq * 256 + (cl & 1) * 128; u.s1 = 16; u.s2 = 64; }
  } else {
    const int i3 = min(xg * 648 + (u0 - 162) * 64 + l, 5183), kg = i3 / 72, cg = i3 % 72, ks = kg >> 2, kq = kg & 3;
    const int xx = kg / 9, uu = kg % 9;
    const int row = uu < 8 ? (4 * xx + (uu >> 1)) * 18 + 8 * (uu & 1) : 4 * xx * 18 + 16;
    u.src = wo + (size_t)row * D_ + cg * 8; u.ld = D_; u.a = uu < 8 ? 4 : 36; u.b = uu < 8 ? 2 : 18; u.c = 1;
    u.p = ws + WS_WO + (size_t)((cg >> 1) * 18 + ks) * 1024 + (kq * 16 + (cg & 1) * 8) * 16;
    u.s1 = 16; u.s2 = 64;
  }
  return u;
}

struct KVt { u32x4 kl; u32x2 kh; };
__global__ __launch_bounds__(512, 2) void k_attn(const u16* __restrict__ wo, const u16* __restrict__ w1, const u16* __restrict__ w2,
                                                 char* __restrict__ ws) {
  extern __shared__ __attribute__((aligned(16))) char lds[];
  const int b = blockIdx.x, tid = threadIdx.x, l = tid & 63;
  const int w = __builtin_amdgcn_readfirstlane(tid >> 6);
  const int xg = b & 7;
  TS(2, 0);
  const int hl = (b >> 3) & 3, head = 4 * xg + hl, qb = b >> 5;
  const int r32 = l & 31, h = l >> 5;
  const char* kvh = ws + WS_KV + (size_t)head * KV_HEAD;
  const int tq = qb * 8 + w;
  u32x4 qlo; unsigned qh0;
  {
    const char* pq = ws + WS_QLO + (size_t)head * 65536 + tq * 1024 + l * 16;
    const char* ph = ws + WS_QHI + (size_t)head * 8192 + (tq * 32 + r32) * 4;
    asm volatile("global_load_dwordx4 %0, %1, off" : "=v"(qlo) : "v"(pq) : "memory");
    asm volatile("global_load_dword %0, %1, off" : "=v"(qh0) : "v"(ph) : "memory");
  }
  const unsigned lds0 = (unsigned)(uintptr_t)lds;
#pragma unroll
  for (int G = 0; G < 4; ++G)
#pragma unroll
    for (int m = 0; m < 5; ++m) {
      const int i = min(w + 8 * m, 38);
      const int off = i < 16 ? (G * 16 + i) * 1024 : (i < 20 ? KV_KHI + G * 4096 + (i - 16) * 1024 : KV_VT + G * 19456 + (i - 20) * 1024);
      glds16(kvh + off + l * 16, (unsigned)__builtin_amdgcn_readfirstlane(lds0 + off));
    }
  const int ru = (b >> 3) * 8 + w;
  const Unit u = retile_unit(min(ru, 172), xg, l, wo, w1, w2, ws);
  asm volatile("s_waitcnt vmcnt(15)\n\ts_barrier" : "+v"(qlo), "+v"(qh0) :: "memory");
  TS(2, 1);
  u32x4 rin[8];
  unit_load(u, rin);
  SB();
  const char* pk_lo = lds + l * 16;
  const char* pk_hi = lds + KV_KHI + r32 * 8;
  const char* pv = lds + KV_VT + h * 304 + min(r32, 18) * 16;
  u32x2 qhi = u32x2{h == 0 ? qh0 : 0u, 0u};
  const f32x16 zero16 = {0, 0, 0, 0, 0, 0, 0, 0, 0, 0, 0, 0, 0, 0, 0, 0};
  f32x16 sA, sB;
  {
    const u32x4 kl0 = *(const u32x4*)pk_lo;
    const u32x2 kh0 = *(const u32x2*)pk_hi;
    f32x16 s0 = mfma32(kl0, qlo, zero16);
    s0 = mfma32k8(kh0, qhi, s0);
    float m = s0[0];
#pragma unroll
    for (int i = 1; i < 16; ++i) m = fmaxf(m, s0[i]);
    auto rr = __builtin_amdgcn_permlane32_swap(__float_as_uint(m), __float_as_uint(m), false, false);
    m = fmaxf(__uint_as_float(rr[0]), __uint_as_float(rr[1]));
    qhi[1] = h == 0 ? (pk2(-m, 0.f) & 0xffffu) : 0u;
    sA = mfma32(kl0, qlo, zero16);
    sA = mfma32k8(kh0, qhi, sA);
  }
  f32x16 o = zero16;
  u32x4 klA = *(const u32x4*)(pk_lo + 1024), klB, vA0 = *(const u32x4*)pv, vA1 = *(const u32x4*)(pv + 608), vB0, vB1;
  u32x2 khA = *(const u32x2*)(pk_hi + 256), khB;
  auto tile = [&](f32x16& sc, f32x16& sn, const u32x4& kl, const u32x2& kh, const u32x4& v0, const u32x4& v1,
                  u32x4& kl2, u32x2& kh2, u32x4& w0, u32x4& w1_, int t) {
    float e[16];
    u32x4 p0, p1;
#pragma unroll
    for (int i = 0; i < 5; ++i) e[i] = fast_exp2(sc[i]);
    SB();
    sn = mfma32(kl, qlo, zero16);
    kl2 = *(const u32x4*)(pk_lo + (t + 2) * 1024);
    kh2 = *(const u32x2*)(pk_hi + (t + 2) * 256);
    w0 = *(const u32x4*)(pv + (t + 1) * VT_TILE);
    w1_ = *(const u32x4*)(pv + (t + 1) * VT_TILE + 608);
    SB();
#pragma unroll
    for (int i = 5; i < 8; ++i) e[i] = fast_exp2(sc[i]);
#pragma unroll
    for (int j = 0; j < 4; ++j) p0[j] = pk2(e[2 * j], e[2 * j + 1]);
    SB();
    o = mfma32(v0, p0, o);
    SB();
#pragma unroll
    for (int i = 8; i < 13; ++i) e[i] = fast_exp2(sc[i]);
    SB();
    sn = mfma32k8(kh, qhi, sn);
    SB();
#pragma unroll
    for (int i = 13; i < 16; ++i) e[i] = fast_exp2(sc[i]);
#pragma unroll
    for (int j = 0; j < 4; ++j) p1[j] = pk2(e[8 + 2 * j], e[8 + 2 * j + 1]);
    SB();
    o = mfma32(v1, p1, o);
    SB();
  };
#pragma unroll
  for (int G = 0; G < 4; ++G) {
    if (G == 1) asm volatile("s_waitcnt vmcnt(18)\n\ts_barrier" ::: "memory");
    if (G == 2) asm volatile("s_waitcnt vmcnt(13)\n\ts_barrier" ::: "memory");
    if (G == 3) asm volatile("s_waitcnt vmcnt(8)\n\ts_barrier" ::: "memory");
    TS(2, 2 + G);
    const int t0 = G == 0 ? 0 : 16 * G - 2, t1 = G == 3 ? 64 : 16 * G + 14;
#pragma unroll 1
    for (int t = t0; t < t1; t += 2) {
      tile(sA, sB, klA, khA, vA0, vA1, klB, khB, vB0, vB1, t);
      tile(sB, sA, klB, khB, vB0, vB1, klA, khA, vA0, vA1, t + 1);
    }
    if (G == 2) {
      if (ru < 173 && (ru < 172 || xg * 648 + 640 + l < xg * 648 + 648)) {
        const __amdgpu_buffer_rsrc_t rs = ws_rsrc(ws);
        const unsigned p = (unsigned)(u.p - ws);
#pragma unroll
        for (int i = 0; i < 8; ++i) st_dev(rs, p + u.s1 * (i & 3) + u.s2 * (i >> 2), unit_col(rin, i));
      }
    }
  }
  TS(2, 6);
  {
    bool bad = false;
#pragma unroll
    for (int i = 0; i < 11; ++i) bad |= !(fabsf(o[i]) < 3.0e38f);
    if (__any(bad)) {
      qhi[1] = 0u;
      float m = -3.0e38f;
      o = zero16;
      for (int t = 0; t < 64; ++t) {
        const u32x4 kl = *(const u32x4*)(pk_lo + t * 1024);
        const u32x2 kh = *(const u32x2*)(pk_hi + t * 256);
        const u32x4 v0 = *(const u32x4*)(pv + t * VT_TILE);
        const u32x4 v1 = *(const u32x4*)(pv + t * VT_TILE + 608);
        f32x16 s = mfma32(kl, qlo, zero16);
        s = mfma32k8(kh, qhi, s);
        float tm = s[0];
#pragma unroll
        for (int i = 1; i < 16; ++i) tm = fmaxf(tm, s[i]);
        auto rr = __builtin_amdgcn_permlane32_swap(__float_as_uint(tm), __float_as_uint(tm), false, false);
        tm = fmaxf(__uint_as_float(rr[0]), __uint_as_float(rr[1]));
        const float mn = fmaxf(m, tm), al = fast_exp2(m - mn);
        m = mn;
#pragma unroll
        for (int i = 0; i < 16; ++i) o[i] *= al;
        u32x4 p0, p1;
#pragma unroll
        for (int j = 0; j < 4; ++j) {
          p0[j] = pk2(fast_exp2(s[2 * j] - m), fast_exp2(s[2 * j + 1] - m));
          p1[j] = pk2(fast_exp2(s[8 + 2 * j] - m), fast_exp2(s[8 + 2 * j + 1] - m));
        }
        o = mfma32(v0, p0, o);
        o = mfma32(v1, p1, o);
      }
    }
  }
  const float inv = 1.f / o[10];
  const unsigned a0 = pk2(o[0] * inv, o[1] * inv), a1 = pk2(o[2] * inv, o[3] * inv);
  const unsigned b0 = pk2(o[4] * inv, o[5] * inv), b1 = pk2(o[6] * inv, o[7] * inv);
  const unsigned tail = pk2(o[8] * inv, o[9] * inv);
  auto r0 = __builtin_amdgcn_permlane32_swap(a0, b0, false, false);
  auto r1 = __builtin_amdgcn_permlane32_swap(a1, b1, false, false);
  const int tok = qb * 256 + w * 32 + r32, mb = tok >> 4, row = tok & 15;
  char* ot = ws + WS_OT + (size_t)mb * 18432 + row * 16;
  const int bi = xg * 9 + 2 * hl + h, bt = xg * 9 + 8;
  *(u32x4*)(ot + (bi >> 2) * 1024 + (bi & 3) * 256) = u32x4{r0[0], r1[0], r0[1], r1[1]};
  if (h == 0) *(unsigned*)(ot + (bt >> 2) * 1024 + (bt & 3) * 256 + hl * 4) = tail;
  TS(2, 7);
}

constexpr int L_H = 0, L_X = 73728, L_STASH = 147456, L_GS = 156672, L_MLP = 161280;

template <int NB, int P0, int P1, int PEND, bool BMAP>
DI void mlp_gemm(const char* __restrict__ wt, const char* A, int l, u32x4 (&wf)[3][NB], f32x4 (&acc)[4][NB]) {
  constexpr int NS = 3;
  auto kmap = [](int p) { return BMAP ? (p < 8 ? p : (p < 10 ? p + 8 : p - 2)) : p; };
  u32x4 hf[2][4];
#pragma unroll
  for (int mb = 0; mb < 4; ++mb) hf[P0 & 1][mb] = *(const u32x4*)(A + mb * 18432 + kmap(P0) * 1024 + l * 16);
#pragma unroll
  for (int p = P0; p < P1; ++p) {
#pragma unroll
    for (int nb = 0; nb < NB; ++nb) {
      if (nb < 4 && p + 1 < P1) hf[(p + 1) & 1][nb] = *(const u32x4*)(A + nb * 18432 + kmap(p + 1) * 1024 + l * 16);
#pragma unroll
      for (int mb = 0; mb < 4; ++mb) acc[mb][nb] = mfma16(wf[p % NS][nb], hf[p & 1][mb], acc[mb][nb]);
      if (p + NS < PEND) wf[p % NS][nb] = *(const u32x4*)(wt + ((p + NS) * NB + nb) * 1024);
      SB();
    }
  }
}

template <int NB>
DI void mlp_body(char* __restrict__ ws, char* lds, const char* __restrict__ wt, u32x4 (&wf)[3][NB], int xg, int r, int s, int l) {
  const int g = l >> 4, n = l & 15;
  f32x4 acc[4][NB];
#pragma unroll
  for (int mb = 0; mb < 4; ++mb)
#pragma unroll
    for (int nb = 0; nb < NB; ++nb) acc[mb][nb] = f32x4{0, 0, 0, 0};
  if (NB == 5) __builtin_amdgcn_s_setprio(2);
  mlp_gemm<NB, 0, 18, 36, false>(wt, lds + L_H, l, wf, acc);
  TS(4, 5);
  if (NB == 4) asm volatile("s_barrier" ::: "memory");
  {
    char* X = lds + L_X;
    const int grp = NB == 5 ? s : 4 + s;
#pragma unroll
    for (int mb = 0; mb < 4; ++mb) {
      u32x2 xp[NB];
#pragma unroll
      for (int nb = 0; nb < NB; ++nb) {
        xp[nb] = u32x2{pk2(silu_f(acc[mb][nb][0]), silu_f(acc[mb][nb][1])), pk2(silu_f(acc[mb][nb][2]), silu_f(acc[mb][nb][3]))};
        acc[mb][nb] = f32x4{0, 0, 0, 0};
      }
#pragma unroll
      for (int p = 0; p < 2; ++p)
        *(u32x4*)(X + (mb * 18 + 2 * grp + p) * 1024 + l * 16) = u32x4{xp[2 * p][0], xp[2 * p][1], xp[2 * p + 1][0], xp[2 * p + 1][1]};
      if (NB == 5) *(u32x2*)(X + (mb * 18 + 16 + (s >> 1)) * 1024 + l * 16 + (s & 1) * 8) = xp[NB - 1];
    }
  }
  TS(4, 6);
  asm volatile("s_waitcnt lgkmcnt(0)\n\ts_barrier" ::: "memory");
  if (NB == 5) {
    mlp_gemm<NB, 0, 10, 18, true>(wt + 18 * NB * 1024, lds + L_X, l, wf, acc);
    asm volatile("s_barrier" ::: "memory");
    mlp_gemm<NB, 10, 18, 18, true>(wt + 18 * NB * 1024, lds + L_X, l, wf, acc);
  } else {
    mlp_gemm<NB, 0, 18, 18, true>(wt + 18 * NB * 1024, lds + L_X, l, wf, acc);
  }
  TS(4, 7);
  const __amdgpu_buffer_rsrc_t rs = ws_rsrc(ws);
  const unsigned pbase = (unsigned)WS_PART + (unsigned)((xg * 32 + r) * 8) * 9216u;
  const int grp = NB == 5 ? s : 4 + s;
#pragma unroll
  for (int mb = 0; mb < 4; ++mb) {
#pragma unroll
    for (int q = 0; q < 4; q += 2) {
      const u32x2 h0 = u32x2{pk2(acc[mb][0][q], acc[mb][1][q]), pk2(acc[mb][2][q], acc[mb][3][q])};
      const u32x2 h1 = u32x2{pk2(acc[mb][0][q + 1], acc[mb][1][q + 1]), pk2(acc[mb][2][q + 1], acc[mb][3][q + 1])};
      const int ch = 8 * grp + 4 * (g & 1) + q + (g >> 1);
      st_dev(rs, pbase + (2 * mb + (n >> 3)) * 9216 + ch * 128 + (n & 7) * 16, join32(h0, h1));
    }
  }
  if (NB == 5) {
#pragma unroll
    for (int mb = 0; mb < 4; mb += 2) {
      const u32x2 h0 = u32x2{pk2(acc[mb][NB - 1][0], acc[mb][NB - 1][1]), pk2(acc[mb][NB - 1][2], acc[mb][NB - 1][3])};
      const u32x2 h1 = u32x2{pk2(acc[mb + 1][NB - 1][0], acc[mb + 1][NB - 1][1]), pk2(acc[mb + 1][NB - 1][2], acc[mb + 1][NB - 1][3])};
      const int ch = 64 + 2 * s + (g >> 1);
      st_dev(rs, pbase + (2 * (mb + (g & 1)) + (n >> 3)) * 9216 + ch * 128 + (n & 7) * 16, join16(h0, h1));
    }
    __builtin_amdgcn_s_setprio(0);
  }
}

__global__ __launch_bounds__(512, 2) void k_mlp(const u16* __restrict__ x, char* __restrict__ ws, u16* __restrict__ out) {
  extern __shared__ __attribute__((aligned(16))) char lds[];
  const int b = blockIdx.x, tid = threadIdx.x, l = tid & 63;
  const int w = __builtin_amdgcn_readfirstlane(tid >> 6);
  const int xg = b & 7, r = b >> 3, s = w & 3;
  const int g = l >> 4, n = l & 15;
  gu32* cnt1 = (gu32*)(ws + WS_FLAG) + r * 64;
  gu32* cnt2 = (gu32*)(ws + WS_FLAG) + (32 + r) * 64;
  gu32* tmo = (gu32*)(ws + WS_FLAG) + 64 * 64;
  const __amdgpu_buffer_rsrc_t rs = ws_rsrc(ws);
  TS(4, 0);
  const int rh = xg & 1, cq = xg >> 1;
  const float* mod = (const float*)(ws + WS_MOD);
  {
    u32x4 ov[5];
    const char* src = ws + WS_OT + (size_t)(4 * r + 2 * rh) * 18432 + l * 16;
#pragma unroll
    for (int i = 0; i < 5; ++i) ov[i] = *(const u32x4*)(src + min(w + 8 * i, 35) * 1024);
    u32x4 wo[18], wo8[3];
    const char* wot = ws + WS_WO + (size_t)(cq * 9 * 18) * 1024 + l * 16;
#pragma unroll
    for (int ks = 0; ks < 18; ++ks) wo[ks] = *(const u32x4*)(wot + (w * 18 + ks) * 1024);
#pragma unroll
    for (int i = 0; i < 3; ++i) wo8[i] = *(const u32x4*)(wot + (8 * 18 + min(w + 8 * i, 17)) * 1024);
    u32x2 xr[2];
#pragma unroll
    for (int mb = 0; mb < 2; ++mb) xr[mb] = *(const u32x2*)(x + (size_t)(r * 64 + rh * 32 + mb * 16 + n) * D_ + cq * 144 + w * 16 + 4 * g);
    const f32x4 gav = *(const f32x4*)(mod + 2 * 576 + cq * 144 + w * 16 + 4 * g);
    u32x2 xr8[2]; f32x4 gav8;
    if (w == 0) {
#pragma unroll
      for (int mb = 0; mb < 2; ++mb) xr8[mb] = *(const u32x2*)(x + (size_t)(r * 64 + rh * 32 + mb * 16 + n) * D_ + cq * 144 + 128 + 4 * g);
      gav8 = *(const f32x4*)(mod + 2 * 576 + cq * 144 + 128 + 4 * g);
    }
    f32x4 gv = {0, 0, 0, 0};
    if (tid < 288) gv = *(const f32x4*)(mod + (tid < 144 ? 4 * 576 + tid * 4 : 3 * 576 + tid * 4 - 576));
    SB();
    char* O = lds + L_X;
#pragma unroll
    for (int i = 0; i < 5; ++i) *(u32x4*)(O + min(w + 8 * i, 35) * 1024 + l * 16) = ov[i];
    if (tid < 288) *(f32x4*)(lds + L_GS + tid * 16) = gv;
    __syncthreads();
    TS(4, 1);
    f32x4 acc[2] = {{0, 0, 0, 0}, {0, 0, 0, 0}}, a8[2] = {{0, 0, 0, 0}, {0, 0, 0, 0}};
#pragma unroll
    for (int i = 0; i < 3; ++i) {
      const int ks = w + 8 * i;
      if (i < 2 || w < 2) {
#pragma unroll
        for (int mb = 0; mb < 2; ++mb) a8[mb] = mfma16(wo8[i], *(const u32x4*)(O + (mb * 18 + ks) * 1024 + l * 16), a8[mb]);
      }
    }
#pragma unroll
    for (int mb = 0; mb < 2; ++mb) *(f32x4*)(O + 36864 + (w * 2 + mb) * 1024 + l * 16) = a8[mb];
#pragma unroll
    for (int ks = 0; ks < 18; ++ks)
#pragma unroll
      for (int mb = 0; mb < 2; ++mb) acc[mb] = mfma16(wo[ks], *(const u32x4*)(O + (mb * 18 + ks) * 1024 + l * 16), acc[mb]);
    auto x1pk = [&](const f32x4& a, const u32x2& xv, const f32x4& gg) {
      return u32x2{pk2(lo2f(xv[0]) + gg[0] * a[0], hi2f(xv[0]) + gg[1] * a[1]), pk2(lo2f(xv[1]) + gg[2] * a[2], hi2f(xv[1]) + gg[3] * a[3])};
    };
    const unsigned x1b = (unsigned)WS_X1 + r * 73728u + (2 * rh + (g & 1)) * 18432u + n * 16;
    st_dev(rs, x1b + (cq * 18 + 2 * w + (g >> 1)) * 256, join16(x1pk(acc[0], xr[0], gav), x1pk(acc[1], xr[1], gav)));
    __syncthreads();
    if (w == 0) {
#pragma unroll
      for (int mb = 0; mb < 2; ++mb) {
        a8[mb] = f32x4{0, 0, 0, 0};
#pragma unroll
        for (int i = 0; i < 8; ++i) a8[mb] += *(const f32x4*)(O + 36864 + (i * 2 + mb) * 1024 + l * 16);
      }
      st_dev(rs, x1b + (cq * 18 + 16 + (g >> 1)) * 256, join16(x1pk(a8[0], xr8[0], gav8), x1pk(a8[1], xr8[1], gav8)));
    }
  }
  TS(4, 2);
  publish(cnt1);
  const char* wt = ws + WS_W12 + (size_t)xg * 1296 * 1024 + (size_t)(w < 4 ? s * 180 : 720 + s * 144) * 1024 + l * 16;
  u32x4 wf5[3][5], wf4[3][4];
  if (w != 0) {
    if (w < 4) {
#pragma unroll
      for (int p = 0; p < 3; ++p)
#pragma unroll
        for (int nb = 0; nb < 5; ++nb) wf5[p][nb] = *(const u32x4*)(wt + (p * 5 + nb) * 1024);
    } else {
#pragma unroll
      for (int p = 0; p < 3; ++p)
#pragma unroll
        for (int nb = 0; nb < 4; ++nb) wf4[p][nb] = *(const u32x4*)(wt + (p * 4 + nb) * 1024);
    }
  }
  SB();
  await(cnt1, 8, tmo);
  TS(4, 3);
  {
    u32x4 xv[9];
    const unsigned src = (unsigned)WS_X1 + r * 73728u + (w >> 1) * 18432u + ((w & 1) * 8 + (l & 7)) * 16 + (l >> 3) * 256;
#pragma unroll
    for (int i = 0; i < 9; ++i) xv[i] = ld_dev(rs, src + i * 2048);
    if (w == 0) {
#pragma unroll
      for (int p = 0; p < 3; ++p)
#pragma unroll
        for (int nb = 0; nb < 5; ++nb) wf5[p][nb] = *(const u32x4*)(wt + (p * 5 + nb) * 1024);
    }
    SB();
    float sm = 0.f, sq = 0.f;
#pragma unroll
    for (int i = 0; i < 9; ++i)
#pragma unroll
      for (int j = 0; j < 4; ++j) { sm = dot2(xv[i][j], 0x3f803f80u, sm); sq = dot2(xv[i][j], xv[i][j], sq); }
    if (w == xg) {
#pragma unroll
      for (int i = 0; i < 9; ++i) *(u32x4*)(lds + L_STASH + ((l >> 3) + 8 * i) * 128 + (l & 7) * 16) = xv[i];
    }
    const float mu = red_sub(sm) * (1.f / D_);
    const float var = fmaxf(red_sub(sq) * (1.f / D_) - mu * mu, 0.f);
    const float rstd = 1.f / sqrtf(var + LN_EPS);
    ln_apply(xv, f32x2{rstd, -mu * rstd}, (const float*)(lds + L_GS), lds + L_H, w, l);
  }
  __syncthreads();
  TS(4, 4);
  if (w < 4) mlp_body<5>(ws, lds, wt, wf5, xg, r, s, l);
  else mlp_body<4>(ws, lds, wt, wf4, xg, r, s, l);
  TS(4, 8);
  publish(cnt2);
  const int i0 = w * 72 + l, i1 = w * 72 + 64 + (l & 7);
  const f32x4 g00 = *(const f32x4*)(mod + 5 * 576 + (i0 >> 3) * 8), g01 = *(const f32x4*)(mod + 5 * 576 + (i0 >> 3) * 8 + 4);
  const f32x4 g10 = *(const f32x4*)(mod + 5 * 576 + (i1 >> 3) * 8), g11 = *(const f32x4*)(mod + 5 * 576 + (i1 >> 3) * 8 + 4);
  await(cnt2, 8, tmo);
  TS(4, 9);
  u32x4 p0[8], p1[8];
  const unsigned pb = (unsigned)WS_PART + (unsigned)(r * 8 + xg) * 9216u;
#pragma unroll
  for (int sl = 0; sl < 8; ++sl) p0[sl] = ld_dev(rs, pb + sl * (32 * 8 * 9216u) + i0 * 16);
  if (l < 8) {
#pragma unroll
    for (int sl = 0; sl < 8; ++sl) p1[sl] = ld_dev(rs, pb + sl * (32 * 8 * 9216u) + i1 * 16);
  }
  auto fin = [&](const u32x4 (&p)[8], int idx, const f32x4& ga, const f32x4& gb) {
    const u32x4 xv = *(const u32x4*)(lds + L_STASH + idx * 16);
    u32x4 o;
#pragma unroll
    for (int j = 0; j < 4; ++j) {
      float a = 0.f, c = 0.f;
#pragma unroll
      for (int sl = 0; sl < 8; ++sl) { a += lo2f(p[sl][j]); c += hi2f(p[sl][j]); }
      const float g0 = j < 2 ? ga[2 * j] : gb[2 * j - 4], g1 = j < 2 ? ga[2 * j + 1] : gb[2 * j - 3];
      o[j] = pk2(lo2f(xv[j]) + g0 * a, hi2f(xv[j]) + g1 * c);
    }
    *(u32x4*)(out + (size_t)(r * 64 + xg * 8 + (idx & 7)) * D_ + (idx >> 3) * 8) = o;
  };
  fin(p0, i0, g00, g01);
  if (l < 8) fin(p1, i1, g10, g11);
  TS(4, 10);
}

extern "C" void kernel_launch(void* const* d_in, const int* in_sizes, int n_in, void* d_out, int out_size, void* d_ws, size_t ws_size,
                              hipStream_t stream) {
  const u16* x = (const u16*)d_in[0];
  const u16* wqkv = (const u16*)d_in[1];
  const u16* wo = (const u16*)d_in[2];
  const u16* w1 = (const u16*)d_in[4];
  const u16* w2 = (const u16*)d_in[5];
  const u16* c = (const u16*)d_in[6];
  const u16* wmod = (const u16*)d_in[7];
  char* ws = (char*)d_ws;
  (void)hipFuncSetAttribute((const void*)k_attn, hipFuncAttributeMaxDynamicSharedMemorySize, 163840);
  (void)hipFuncSetAttribute((const void*)k_mlp, hipFuncAttributeMaxDynamicSharedMemorySize, L_MLP);
  k_prep<<<256, 448, 0, stream>>>(x, wqkv, c, wmod, ws);
  k_qkv<<<256, 512, 0, stream>>>(x, ws);
  k_attn<<<256, 512, 163840, stream>>>(wo, w1, w2, ws);
  k_mlp<<<256, 512, L_MLP, stream>>>(x, ws, (u16*)d_out);
}
```

```cpp
#include <hip/hip_runtime.h>
#include <hip/hip_bf16.h>
#include <stdint.h>
#include <type_traits>

typedef __attribute__((ext_vector_type(8))) __bf16 bf16x8;
typedef __attribute__((ext_vector_type(2))) __bf16 bf16x2;
typedef __attribute__((ext_vector_type(4))) short s16x4;
typedef __attribute__((ext_vector_type(2))) float f32x2;
typedef __attribute__((ext_vector_type(4))) float f32x4;
typedef __attribute__((ext_vector_type(16))) float f32x16;
typedef __attribute__((ext_vector_type(2))) unsigned u32x2;
typedef __attribute__((ext_vector_type(4))) unsigned u32x4;
typedef unsigned short u16;
typedef __attribute__((address_space(1))) unsigned gu32;

#define DI __device__ __forceinline__
#define SB() __builtin_amdgcn_sched_barrier(0)

constexpr int S_ = 2048, D_ = 576, F_ = 4608;
constexpr float LN_EPS = 1e-5f;
constexpr float QSCALE = 0.23570226039551584f * 1.4426950408889634f;

constexpr size_t WS_FLAG = 0;
constexpr size_t WS_MOD  = 20480;
constexpr size_t WS_STAT = 36864;
constexpr size_t WS_WQKV = 53248;
constexpr size_t WS_QLO  = WS_WQKV + 2064384;
constexpr size_t WS_QHI  = WS_QLO + 2097152;
constexpr int    KV_HEAD = 159744, KV_KHI = 65536, KV_VT = 81920, VT_TILE = 1216;
constexpr size_t WS_KV   = WS_QHI + 262144;
constexpr size_t WS_OT   = WS_KV + 32 * (size_t)KV_HEAD;
constexpr size_t WS_WO   = WS_OT + 2359296;
constexpr size_t WS_W12  = WS_WO + 663552;
constexpr size_t WS_X1   = WS_W12 + 2 * 5308416;
constexpr size_t WS_PART = WS_X1 + 2359296;
constexpr size_t WS_END  = WS_PART + 18874368;

#define TS(K, I)

DI float bf2f(u16 v) { return __uint_as_float((unsigned)v << 16); }
DI float lo2f(unsigned v) { return __uint_as_float(v << 16); }
DI float hi2f(unsigned v) { return __uint_as_float(v & 0xffff0000u); }
DI unsigned pk2(float a, float b) { bf16x2 v = {(__bf16)a, (__bf16)b}; return __builtin_bit_cast(unsigned, v); }
DI float rbf(float a) { return (float)(__bf16)a; }
DI f32x4 mfma16(u32x4 a, u32x4 b, f32x4 c) {
  return __builtin_amdgcn_mfma_f32_16x16x32_bf16(__builtin_bit_cast(bf16x8, a), __builtin_bit_cast(bf16x8, b), c, 0, 0, 0);
}
DI f32x16 mfma32(u32x4 a, u32x4 b, f32x16 c) {
  return __builtin_amdgcn_mfma_f32_32x32x16_bf16(__builtin_bit_cast(bf16x8, a), __builtin_bit_cast(bf16x8, b), c, 0, 0, 0);
}
DI f32x16 mfma32k8(u32x2 a, u32x2 b, f32x16 c) {
  return __builtin_amdgcn_mfma_f32_32x32x8bf16_1k(__builtin_bit_cast(s16x4, a), __builtin_bit_cast(s16x4, b), c, 0, 0, 0);
}
DI float fast_rcp(float x) { return __builtin_amdgcn_rcpf(x); }
DI float fast_exp2(float x) { return __builtin_amdgcn_exp2f(x); }
DI float silu_f(float x) { return x * fast_rcp(1.f + fast_exp2(-1.4426950408889634f * x)); }
DI float xor_add(float v, int m) { return v + __shfl_xor(v, m); }
DI float dot2(unsigned a, unsigned b, float c) {
  return __builtin_amdgcn_fdot2_f32_bf16(__builtin_bit_cast(bf16x2, a), __builtin_bit_cast(bf16x2, b), c, false);
}
DI float red_sub(float v) {
  v += __builtin_bit_cast(float, __builtin_amdgcn_update_dpp(0, __builtin_bit_cast(int, v), 0x128, 0xf, 0xf, false));
  auto a = __builtin_amdgcn_permlane16_swap(__float_as_uint(v), __float_as_uint(v), false, false);
  v = __uint_as_float(a[0]) + __uint_as_float(a[1]);
  auto b = __builtin_amdgcn_permlane32_swap(__float_as_uint(v), __float_as_uint(v), false, false);
  return __uint_as_float(b[0]) + __uint_as_float(b[1]);
}
DI u32x4 join16(u32x2 x, u32x2 y) {
  auto a = __builtin_amdgcn_permlane16_swap(x[0], y[0], false, false);
  auto b = __builtin_amdgcn_permlane16_swap(x[1], y[1], false, false);
  return u32x4{a[0], b[0], a[1], b[1]};
}
DI u32x4 join32(u32x2 x, u32x2 y) {
  auto a = __builtin_amdgcn_permlane32_swap(x[0], y[0], false, false);
  auto b = __builtin_amdgcn_permlane32_swap(x[1], y[1], false, false);
  return u32x4{a[0], b[0], a[1], b[1]};
}
DI void glds16(const void* g, unsigned lds_base) {
  unsigned sv;
  asm volatile("s_mov_b32 %0, m0\n\ts_mov_b32 m0, %2\n\ts_nop 0\n\tglobal_load_lds_dwordx4 %1, off\n\ts_mov_b32 m0, %0"
               : "=&s"(sv) : "v"(g), "s"(lds_base) : "memory");
}
DI __amdgpu_buffer_rsrc_t ws_rsrc(char* ws) { return __builtin_amdgcn_make_buffer_rsrc(ws, 0, (int)WS_END, 0x00020000); }
DI void st_dev(__amdgpu_buffer_rsrc_t rs, unsigned off, u32x4 v) { __builtin_amdgcn_raw_buffer_store_b128(v, rs, off, 0, 16); }
DI u32x4 ld_dev(__amdgpu_buffer_rsrc_t rs, unsigned off) { return __builtin_amdgcn_raw_buffer_load_b128(rs, off, 0, 16); }
DI void publish(gu32* cnt) {
  asm volatile("s_waitcnt vmcnt(0)" ::: "memory");
  __syncthreads();
  if (threadIdx.x == 0) __hip_atomic_fetch_add(cnt, 1u, __ATOMIC_RELAXED, __HIP_MEMORY_SCOPE_AGENT);
}
DI void await(gu32* cnt, unsigned want, gu32* tmo) {
  if (threadIdx.x == 0) {
    for (unsigned spins = 0; __hip_atomic_load(cnt, __ATOMIC_RELAXED, __HIP_MEMORY_SCOPE_AGENT) < want;) {
      __builtin_amdgcn_s_sleep(1);
      if (++spins > (1u << 20)) { __hip_atomic_store(tmo, 1u, __ATOMIC_RELAXED, __HIP_MEMORY_SCOPE_AGENT); break; }
    }
  }
  __builtin_amdgcn_fence(__ATOMIC_ACQUIRE, "wavefront");
  __syncthreads();
}

struct Unit { const u16* src; char* p; int ld, a, b, c, s1, s2; };
DI void unit_load(const Unit& u, u32x4 (&in)[8]) {
#pragma unroll
  for (int j = 0; j < 8; ++j) in[j] = *(const u32x4*)(u.src + (size_t)(u.a * (j >> 2) + u.b * ((j >> 1) & 1) + u.c * (j & 1)) * u.ld);
}
DI u32x4 unit_col(const u32x4 (&in)[8], int i) {
  u32x4 o;
#pragma unroll
  for (int m = 0; m < 4; ++m) o[m] = __builtin_amdgcn_perm(in[2 * m + 1][i >> 1], in[2 * m][i >> 1], (i & 1) ? 0x07060302u : 0x05040100u);
  return o;
}

__global__ __launch_bounds__(448) void k_prep(const u16* __restrict__ x, const u16* __restrict__ wqkv, const u16* __restrict__ c,
                                              const u16* __restrict__ wmod, char* __restrict__ ws) {
  __shared__ float red[128 * 16];
  const int b = blockIdx.x, tid = threadIdx.x, l = tid & 63;
  const int w = __builtin_amdgcn_readfirstlane(tid >> 6);
  TS(0, 0);
  if (w < 4) {
    if (b >= 216) {
      if (b == 255) for (int i = tid; i < 5120; i += 256) ((unsigned*)(ws + WS_FLAG))[i] = 0u;
      return;
    }
    const int cg = (b & 7) * 27 + (b >> 3);
    const int half = tid & 1, row0 = tid >> 1;
    u32x4 wv[5]; u16 cv[5];
#pragma unroll
    for (int i = 0; i < 5; ++i) {
      const int k = min(row0 + 128 * i, 575);
      wv[i] = *(const u32x4*)(wmod + (size_t)k * 3456 + cg * 16 + half * 8);
      cv[i] = c[k];
    }
    float acc[8];
#pragma unroll
    for (int j = 0; j < 8; ++j) acc[j] = 0.f;
#pragma unroll
    for (int i = 0; i < 5; ++i) {
      float s = rbf(silu_f(bf2f(cv[i])));
      if (row0 + 128 * i >= 576) s = 0.f;
#pragma unroll
      for (int j = 0; j < 4; ++j) { acc[2 * j] += s * lo2f(wv[i][j]); acc[2 * j + 1] += s * hi2f(wv[i][j]); }
    }
    *(f32x4*)&red[row0 * 16 + half * 8] = f32x4{acc[0], acc[1], acc[2], acc[3]};
    *(f32x4*)&red[row0 * 16 + half * 8 + 4] = f32x4{acc[4], acc[5], acc[6], acc[7]};
    __syncthreads();
    if (w == 0) {
      const int col = l & 15, part = l >> 4;
      float s = 0.f;
#pragma unroll
      for (int i = 0; i < 32; ++i) s += red[(part * 32 + i) * 16 + col];
      s = xor_add(s, 16); s = xor_add(s, 32);
      if (l < 16) {
        const int n = cg * 16 + col, sec = n / 576;
        float v = rbf(s);
        if (sec == 1 || sec == 4) v = rbf(1.f + v);
        ((float*)(ws + WS_MOD))[n] = v;
      }
    }
    TS(0, 1);
  } else if (w == 4) {
    const int row = 8 * b + (l & 7), sub = l >> 3;
    u32x4 xv[9];
#pragma unroll
    for (int i = 0; i < 9; ++i) xv[i] = *(const u32x4*)(x + (size_t)row * D_ + (sub + 8 * i) * 8);
    float s = 0.f;
#pragma unroll
    for (int i = 0; i < 9; ++i)
#pragma unroll
      for (int j = 0; j < 4; ++j) s += lo2f(xv[i][j]) + hi2f(xv[i][j]);
    const float mu = red_sub(s) * (1.f / D_);
    float q = 0.f;
#pragma unroll
    for (int i = 0; i < 9; ++i)
#pragma unroll
      for (int j = 0; j < 4; ++j) { const float a = lo2f(xv[i][j]) - mu, bb = hi2f(xv[i][j]) - mu; q += a * a + bb * bb; }
    q = red_sub(q);
    const float rstd = 1.f / sqrtf(q * (1.f / D_) + LN_EPS);
    if (sub == 0) ((f32x2*)(ws + WS_STAT))[row] = f32x2{rstd, -mu * rstd};
    TS(0, 1);
  } else {
    const int xg = b & 7, rank = b >> 3;
    char* dst = ws + WS_WQKV + (size_t)xg * 252 * 1024;
    if (w == 5) {
      if (l < 54) {
        const int i = rank * 54 + l, kg = i / 24, rem = i % 24, part = rem >> 3, hl = (rem >> 1) & 3, half = rem & 1;
        Unit u;
        u.src = wqkv + (size_t)kg * 8 * 1728 + part * 576 + (4 * xg + hl) * 18 + half * 8;
        u.ld = 1728; u.a = 4; u.b = 2; u.c = 1;
        char* p = dst + ((kg >> 2) * 14 + (part < 2 ? 2 * hl + part : 8 + hl)) * 1024 + ((kg & 3) * 16 + half * 8) * 16;
        u32x4 in[8];
        unit_load(u, in);
#pragma unroll
        for (int i2 = 0; i2 < 8; ++i2) *(u32x4*)(p + i2 * 16) = unit_col(in, i2);
      }
    } else {
      if (l < 9) {
        const int i = rank * 9 + l, kg = i >> 2, hl = i & 3;
        const u16* src = wqkv + (size_t)kg * 8 * 1728 + (4 * xg + hl) * 18 + 16;
        unsigned in[3][8];
#pragma unroll
        for (int p = 0; p < 3; ++p)
#pragma unroll
          for (int j = 0; j < 8; ++j) in[p][j] = *(const unsigned*)(src + (size_t)j * 1728 + p * 576);
        char* pq = dst + ((kg >> 2) * 14 + 12) * 1024 + (kg & 3) * 256;
#pragma unroll
        for (int p = 0; p < 3; ++p)
#pragma unroll
          for (int e = 0; e < 2; ++e) {
            u32x4 o;
#pragma unroll
            for (int m = 0; m < 4; ++m) o[m] = __builtin_amdgcn_perm(in[p][2 * m + 1], in[p][2 * m], e ? 0x07060302u : 0x05040100u);
            if (p < 2) *(u32x4*)(pq + (4 * hl + 2 * p + e) * 16) = o;
            else { *(u32x4*)(pq + 1024 + (2 * hl + e) * 16) = o; *(u32x4*)(pq + 1024 + (8 + 2 * hl + e) * 16) = u32x4{0, 0, 0, 0}; }
          }
      }
    }
    TS(0, 1);
  }
}

DI void ln_apply(const u32x4 (&xv)[9], f32x2 st, const float* gs, char* A, int w, int l) {
  const int sub = l >> 3;
  f32x4 gq[2][4];
  auto fetch = [&](int i, f32x4 (&q)[4]) {
    const float* p = gs + (sub + 8 * i) * 8;
    q[0] = *(const f32x4*)p; q[1] = *(const f32x4*)(p + 4); q[2] = *(const f32x4*)(p + 576); q[3] = *(const f32x4*)(p + 580);
  };
  fetch(0, gq[0]);
#pragma unroll
  for (int i = 0; i < 9; ++i) {
    if (i + 1 < 9) fetch(i + 1, gq[(i + 1) & 1]);
    SB();
    const f32x4 (&q)[4] = gq[i & 1];
    u32x4 o;
#pragma unroll
    for (int j = 0; j < 4; ++j) {
      const float ga = q[j >> 1][(2 * j) & 3], gb = q[j >> 1][(2 * j + 1) & 3];
      const float sa = q[2 + (j >> 1)][(2 * j) & 3], sb = q[2 + (j >> 1)][(2 * j + 1) & 3];
      const float a = (lo2f(xv[i][j]) * st[0] + st[1]) * ga + sa;
      const float bb = (hi2f(xv[i][j]) * st[0] + st[1]) * gb + sb;
      o[j] = pk2(a, bb);
    }
    *(u32x4*)(A + (w >> 1) * 18432 + (sub + 8 * i) * 256 + ((w & 1) * 8 + (l & 7)) * 16) = o;
    SB();
  }
}

template <bool TFORM>
DI void qkv_gemm(const char* __restrict__ wt, const char* A, int s, int l, f32x4 (&a0)[4], f32x4 (&a1)[4], f32x4& at,
                 u32x4 (&wf)[6][3]) {
  constexpr int NS = 6;
  u32x4 hf[2][4];
#pragma unroll
  for (int i = 0; i < 4; ++i) hf[0][i] = *(const u32x4*)(A + ((i + s) & 3) * 18432 + l * 16);
#pragma unroll
  for (int ks = 0; ks < 18; ++ks) {
    const int kk = ks % NS;
#pragma unroll
    for (int i = 0; i < 4; ++i) {
      if (ks + 1 < 18) hf[(ks + 1) & 1][i] = *(const u32x4*)(A + ((i + s) & 3) * 18432 + (ks + 1) * 1024 + l * 16);
      if (TFORM) {
        a0[i] = mfma16(wf[kk][0], hf[ks & 1][i], a0[i]);
        a1[i] = mfma16(wf[kk][1], hf[ks & 1][i], a1[i]);
        if (i == 0) at = mfma16(wf[kk][2], hf[ks & 1][0], at);
      } else {
        a0[i] = mfma16(hf[ks & 1][i], wf[kk][0], a0[i]);
        if (i == 0) at = mfma16(hf[ks & 1][0], wf[kk][2], at);
      }
      SB();
    }
    if (ks + NS < 18) {
      wf[kk][0] = *(const u32x4*)(wt + (ks + NS) * 14336);
      if (TFORM) wf[kk][1] = *(const u32x4*)(wt + (ks + NS) * 14336 + 1024);
      wf[kk][2] = *(const u32x4*)(wt + (ks + NS) * 14336 + (TFORM ? 12 - 2 * s : 13 - 8 - s) * 1024);
    }
    SB();
  }
}

__global__ __launch_bounds__(512, 2) void k_qkv(const u16* __restrict__ x, char* __restrict__ ws) {
  __shared__ __attribute__((aligned(16))) char A[73728];
  __shared__ __attribute__((aligned(16))) float gs[1152];
  const int b = blockIdx.x, tid = threadIdx.x, l = tid & 63;
  const int w = __builtin_amdgcn_readfirstlane(tid >> 6);
  const int xg = (b & 6) + ((b >> 3) & 1), r = 2 * (b >> 4) + (b & 1), s = w & 3;
  TS(1, 0);
  u32x4 xv[9];
  const int row = r * 64 + 8 * w + (l & 7);
#pragma unroll
  for (int i = 0; i < 9; ++i) xv[i] = *(const u32x4*)(x + (size_t)row * D_ + ((l >> 3) + 8 * i) * 8);
  const f32x2 st = ((const f32x2*)(ws + WS_STAT))[row];
  f32x4 gv = {0, 0, 0, 0};
  if (tid < 288) gv = *(const f32x4*)((const float*)(ws + WS_MOD) + (tid < 144 ? 576 + tid * 4 : tid * 4 - 576));
  asm volatile("s_barrier" ::: "memory");
  const char* wt = ws + WS_WQKV + (size_t)xg * 252 * 1024 + (w < 4 ? 2 * s : 8 + s) * 1024 + l * 16;
  u32x4 wf[6][3];
#pragma unroll
  for (int ks = 0; ks < 6; ++ks) {
    wf[ks][0] = *(const u32x4*)(wt + ks * 14336);
    if (w < 4) wf[ks][1] = *(const u32x4*)(wt + ks * 14336 + 1024);
    wf[ks][2] = *(const u32x4*)(wt + ks * 14336 + (w < 4 ? 12 - 2 * s : 13 - 8 - s) * 1024);
  }
  SB();
  if (tid < 288) *(f32x4*)(gs + tid * 4) = gv;
  __syncthreads();
  TS(1, 7);
  ln_apply(xv, st, gs, A, w, l);
  TS(1, 1);
  __syncthreads();
  TS(1, 2);
  f32x4 a0[4], a1[4], at = {0, 0, 0, 0};
#pragma unroll
  for (int i = 0; i < 4; ++i) { a0[i] = at; a1[i] = at; }
  const int g = l >> 4, n = l & 15;
  const int head = 4 * xg + s;
  char* kvh = ws + WS_KV + (size_t)head * KV_HEAD;
  if (w < 4) {
    qkv_gemm<true>(wt, A, s, l, a0, a1, at, wf);
    TS(1, 3);
    char* qlo = ws + WS_QLO + (size_t)head * 65536;
#pragma unroll
    for (int i = 0; i < 4; i += 2) {
      const int mb = (i + (g & 1) + s) & 3;
      const int off = (2 * r + (mb >> 1)) * 1024 + (g >> 1) * 512 + (16 * (mb & 1) + n) * 16;
      u32x2 q0, q1, k0, k1;
      q0 = u32x2{pk2(a0[i][0] * QSCALE, a0[i][1] * QSCALE), pk2(a0[i][2] * QSCALE, a0[i][3] * QSCALE)};
      q1 = u32x2{pk2(a0[i + 1][0] * QSCALE, a0[i + 1][1] * QSCALE), pk2(a0[i + 1][2] * QSCALE, a0[i + 1][3] * QSCALE)};
      k0 = u32x2{pk2(a1[i][0], a1[i][1]), pk2(a1[i][2], a1[i][3])};
      k1 = u32x2{pk2(a1[i + 1][0], a1[i + 1][1]), pk2(a1[i + 1][2], a1[i + 1][3])};
      *(u32x4*)(qlo + off) = join16(q0, q1);
      *(u32x4*)(kvh + off) = join16(k0, k1);
    }
    const int tok = r * 64 + s * 16 + n, hg = 4 * xg + g;
    ((unsigned*)(ws + WS_QHI))[hg * 2048 + tok] = pk2(at[0] * QSCALE, at[1] * QSCALE);
    *(u32x2*)(ws + WS_KV + (size_t)hg * KV_HEAD + KV_KHI + tok * 8) = u32x2{pk2(at[2], at[3]), 0x00003f80u};
  } else {
    qkv_gemm<false>(wt, A, s, l, a0, a1, at, wf);
    TS(1, 3);
#pragma unroll
    for (int i = 0; i < 4; i += 2) {
      const int mb = (i + (g >> 1) + s) & 3;
      const u32x2 v0 = u32x2{pk2(a0[i][0], a0[i][1]), pk2(a0[i][2], a0[i][3])};
      const u32x2 v1 = u32x2{pk2(a0[i + 1][0], a0[i + 1][1]), pk2(a0[i + 1][2], a0[i + 1][3])};
      *(u32x4*)(kvh + KV_VT + (2 * r + (mb >> 1)) * VT_TILE + (mb & 1) * 608 + (g & 1) * 304 + n * 16) = join32(v0, v1);
    }
    if (n < 12) {
      const int hg = 4 * xg + (n < 8 ? n >> 1 : n - 8), vr = n < 8 ? 16 + (n & 1) : 18;
      const u32x2 v = n < 8 ? u32x2{pk2(at[0], at[1]), pk2(at[2], at[3])} : u32x2{0x3f803f80u, 0x3f803f80u};
      *(u32x2*)(ws + WS_KV + (size_t)hg * KV_HEAD + KV_VT + (2 * r + (s >> 1)) * VT_TILE + (s & 1) * 608 + (g & 1) * 304 + vr * 16 + (g >> 1) * 8) = v;
    }
  }
  TS(1, 4);
}

DI size_t w12_frag(int grp, int j, int step) {
  return grp < 4 ? grp * 180 + step * 5 + j : (grp < 8 ? 720 + (grp - 4) * 144 + step * 4 + j : j * 180 + step * 5 + 4);
}
DI Unit retile_unit(int u0, int xg, int l, const u16* __restrict__ wo, const u16* __restrict__ w1, const u16* __restrict__ w2, char* __restrict__ ws) {
  Unit u;
  const int cl = l & 7, kgl = l >> 3;
  char* w12 = ws + WS_W12 + (size_t)xg * 1296 * 1024;
  if (u0 < 81) {
    const int grp = u0 / 9, kg = (u0 % 9) * 8 + kgl, ks = kg >> 2, kq = kg & 3;
    u.src = w1 + (size_t)kg * 8 * F_ + xg * 576 + grp * 64 + cl * 8; u.ld = F_; u.a = 4; u.b = 2; u.c = 1;
    u.p = w12 + w12_frag(grp, 0, ks) * 1024 + kq * 256 + cl * 16;
    u.s1 = grp < 8 ? 1024 : 180 * 1024; u.s2 = 128;
  } else if (u0 < 162) {
    const int u2 = u0 - 81, grp = u2 / 9, kg = (u2 % 9) * 8 + kgl, ks = kg >> 2, kq = kg & 3;
    const int row = (ks >> 1) * 64 + 32 * (kq & 1) + 4 * (kq >> 1) + 2 * (ks & 1);
    const int step = 18 + (ks < 8 ? ks : (ks < 16 ? ks + 2 : ks - 8));
    u.src = w2 + (size_t)(xg * 576 + row) * D_ + grp * 64 + cl * 8; u.ld = D_; u.a = 1; u.b = 16; u.c = 8;
    if (grp < 8) { u.p = w12 + w12_frag(grp, 0, step) * 1024 + kq * 256 + cl * 16; u.s1 = 1024; u.s2 = 128; }
    else { u.p = w12 + w12_frag(8, cl >> 1, step) * 1024 + kq * 256 + (cl & 1) * 128; u.s1 = 16; u.s2 = 64; }
  } else {
    const int i3 = min(xg * 648 + (u0 - 162) * 64 + l, 5183), kg = i3 / 72, cg = i3 % 72, ks = kg >> 2, kq = kg & 3;
    const int xx = kg / 9, uu = kg % 9;
    const int row = uu < 8 ? (4 * xx + (uu >> 1)) * 18 + 8 * (uu & 1) : 4 * xx * 18 + 16;
    u.src = wo + (size_t)row * D_ + cg * 8; u.ld = D_; u.a = uu < 8 ? 4 : 36; u.b = uu < 8 ? 2 : 18; u.c = 1;
    u.p = ws + WS_WO + (size_t)((cg >> 1) * 18 + ks) * 1024 + (kq * 16 + (cg & 1) * 8) * 16;
    u.s1 = 16; u.s2 = 64;
  }
  return u;
}

constexpr int L_ZERO = 160768;
typedef __attribute__((address_space(3))) u32x4 lds_u32x4;
typedef __attribute__((address_space(3))) u32x2 lds_u32x2;
DI u32x4 ldsr4(unsigned a) { return *(const lds_u32x4*)(uintptr_t)a; }
DI u32x2 ldsr2(unsigned a) { return *(const lds_u32x2*)(uintptr_t)a; }

__global__ __launch_bounds__(1024, 4) void k_attn(const u16* __restrict__ wo, const u16* __restrict__ w1, const u16* __restrict__ w2,
                                                 char* __restrict__ ws) {
  extern __shared__ __attribute__((aligned(16))) char lds[];
  const int b = blockIdx.x, tid = threadIdx.x, l = tid & 63;
  const int w = __builtin_amdgcn_readfirstlane(tid >> 6);
  const int xg = b & 7;
  TS(2, 0);
  const int hl = (b >> 3) & 3, head = 4 * xg + hl, qb = b >> 5;
  const int r32 = l & 31, h = l >> 5;
  const int qp = w & 3, kh = w >> 2;
  const char* kvh = ws + WS_KV + (size_t)head * KV_HEAD;
  const int tq = qb * 8 + 2 * qp;
  u32x4 qla, qlb; unsigned qha, qhb;
  {
    const char* pq = ws + WS_QLO + (size_t)head * 65536 + tq * 1024 + l * 16;
    const char* ph = ws + WS_QHI + (size_t)head * 8192 + (tq * 32 + r32) * 4;
    asm volatile("global_load_dwordx4 %0, %1, off" : "=v"(qla) : "v"(pq) : "memory");
    asm volatile("global_load_dwordx4 %0, %1, off offset:1024" : "=v"(qlb) : "v"(pq) : "memory");
    asm volatile("global_load_dword %0, %1, off" : "=v"(qha) : "v"(ph) : "memory");
    asm volatile("global_load_dword %0, %1, off offset:128" : "=v"(qhb) : "v"(ph) : "memory");
  }
  const unsigned lds0 = (unsigned)(uintptr_t)lds;
#pragma unroll
  for (int G = 0; G < 2; ++G)
#pragma unroll
    for (int m = 0; m < 5; ++m) {
      const int i = w + 16 * m;
      int off;
      if (m < 2) off = ((i >> 3) * 16 + 8 * G + (i & 7)) * 1024;
      else if (i < 40) off = KV_KHI + (((i - 32) >> 1) * 16 + 8 * G) * 256 + ((i - 32) & 1) * 1024;
      else off = KV_VT + (((i - 40) / 10) * 16 + 8 * G) * VT_TILE + ((i - 40) % 10) * 1024;
      glds16(kvh + off + l * 16, (unsigned)__builtin_amdgcn_readfirstlane(lds0 + off));
    }
  if (tid < 128) *(u32x4*)(lds + L_ZERO + tid * 16) = u32x4{0, 0, 0, 0};
  asm volatile("s_waitcnt vmcnt(5) lgkmcnt(0)\n\ts_barrier" : "+v"(qla), "+v"(qlb), "+v"(qha), "+v"(qhb) :: "memory");
  TS(2, 1);
  {
    const int ru = (b >> 3) * 8 + (w - 8);
    if (w >= 8 && ru < 173) {
      const Unit u = retile_unit(ru, xg, l, wo, w1, w2, ws);
      u32x4 rin[8];
      unit_load(u, rin);
      if (ru < 153) {
        const __amdgpu_buffer_rsrc_t rs = ws_rsrc(ws);
        const unsigned p = (unsigned)(u.p - ws);
#pragma unroll
        for (int i = 0; i < 8; ++i) st_dev(rs, p + u.s1 * (i & 3) + u.s2 * (i >> 2), unit_col(rin, i));
      } else if (ru < 172 || l < 8) {
#pragma unroll
        for (int i = 0; i < 8; ++i) *(u32x4*)(u.p + u.s1 * (i & 3) + u.s2 * (i >> 2)) = unit_col(rin, i);
      }
    }
  }
  SB();
  const bool vz = r32 > 18;
  unsigned ak = lds0 + l * 16, ah = lds0 + KV_KHI + r32 * 8, av = lds0 + KV_VT + h * 304 + r32 * 16;
  const unsigned sv = vz ? 0u : (unsigned)VT_TILE, sh = h ? 0u : 256u;
  u32x2 qa = u32x2{h == 0 ? qha : 0u, 0u}, qb2 = u32x2{h == 0 ? qhb : 0u, 0u};
  const f32x16 zero16 = {0, 0, 0, 0, 0, 0, 0, 0, 0, 0, 0, 0, 0, 0, 0, 0};
  auto rowmax = [&](const f32x16& s0) {
    float m = s0[0];
#pragma unroll
    for (int i = 1; i < 16; ++i) m = fmaxf(m, s0[i]);
    auto rr = __builtin_amdgcn_permlane32_swap(__float_as_uint(m), __float_as_uint(m), false, false);
    return fmaxf(__uint_as_float(rr[0]), __uint_as_float(rr[1]));
  };
  {
    const u32x4 kl0 = ldsr4(ak);
    const u32x2 kh0 = ldsr2(ah);
    f32x16 s0 = mfma32k8(kh0, qa, mfma32(kl0, qla, zero16));
    f32x16 s1 = mfma32k8(kh0, qb2, mfma32(kl0, qlb, zero16));
    const float ma = rowmax(s0), mb = rowmax(s1);
    qa[1] = h == 0 ? (pk2(-ma, 0.f) & 0xffffu) : 0u;
    qb2[1] = h == 0 ? (pk2(-mb, 0.f) & 0xffffu) : 0u;
  }
  ak += kh * 16 * 1024; ah += kh * 16 * 256; av += kh * 16 * VT_TILE;
  if (vz) av = lds0 + L_ZERO;
  if (h) ah = lds0 + L_ZERO;
  asm volatile("" : "+v"(ak), "+v"(ah), "+v"(av));
  u32x4 kl = ldsr4(ak), v0 = ldsr4(av), v1 = ldsr4(av + 608);
  u32x2 kt = ldsr2(ah);
  f32x16 sa = mfma32k8(kt, qa, mfma32(kl, qla, zero16)), sb;
  f32x16 oa = zero16, ob = zero16;
  auto phase = [&](const f32x16& sc, f32x16& sn, f32x16& o, const u32x4& qn, const u32x2& qt, auto first) {
    constexpr bool F = decltype(first)::value;
    float e[16];
    u32x4 p0, p1;
#pragma unroll
    for (int i = 0; i < 5; ++i) e[i] = fast_exp2(sc[i]);
    SB();
    sn = mfma32(kl, qn, zero16);
    if (F) kl = ldsr4(ak + 1024);
    SB();
#pragma unroll
    for (int i = 5; i < 8; ++i) e[i] = fast_exp2(sc[i]);
#pragma unroll
    for (int j2 = 0; j2 < 4; ++j2) p0[j2] = pk2(e[2 * j2], e[2 * j2 + 1]);
    SB();
    o = mfma32(v0, p0, o);
    if (!F) v0 = ldsr4(av + VT_TILE);
    SB();
#pragma unroll
    for (int i = 8; i < 13; ++i) e[i] = fast_exp2(sc[i]);
    SB();
    sn = mfma32k8(kt, qt, sn);
    if (F) kt = ldsr2(ah + 256);
    SB();
#pragma unroll
    for (int i = 13; i < 16; ++i) e[i] = fast_exp2(sc[i]);
#pragma unroll
    for (int j2 = 0; j2 < 4; ++j2) p1[j2] = pk2(e[8 + 2 * j2], e[8 + 2 * j2 + 1]);
    SB();
    o = mfma32(v1, p1, o);
    if (!F) v1 = ldsr4(av + VT_TILE + 608);
    SB();
  };
#pragma unroll
  for (int G = 0; G < 2; ++G) {
    if (G == 1) asm volatile("s_waitcnt vmcnt(0)\n\ts_barrier" ::: "memory");
    TS(2, 2 + G);
    const int n = G == 0 ? 7 : 9;
#pragma unroll 1
    for (int i = 0; i < n; ++i) {
      phase(sa, sb, oa, qlb, qb2, std::true_type{});
      phase(sb, sa, ob, qla, qa, std::false_type{});
      ak += 1024; ah += sh; av += sv;
    }
  }
  TS(2, 6);
  asm volatile("s_barrier" ::: "memory");
  {
    float* ex = (float*)lds + (w * 22) * 64 + l;
#pragma unroll
    for (int i = 0; i < 11; ++i) { if (kh != 0) ex[i * 64] = oa[i]; if (kh != 1) ex[(11 + i) * 64] = ob[i]; }
  }
  __syncthreads();
  if (kh >= 2) return;
  f32x16 o = kh ? ob : oa;
#pragma unroll
  for (int q = 1; q < 4; ++q) {
    const float* ex = (const float*)lds + ((((kh + q) & 3) * 4 + qp) * 22 + kh * 11) * 64 + l;
#pragma unroll
    for (int i = 0; i < 11; ++i) o[i] += ex[i * 64];
  }
  {
    bool bad = false;
#pragma unroll
    for (int i = 0; i < 11; ++i) bad |= !(fabsf(o[i]) < 3.0e38f);
    if (__any(bad)) {
      const u32x4 ql = kh ? qlb : qla;
      const u32x2 qt = u32x2{kh ? qb2[0] : qa[0], 0u};
      float m = -3.0e38f;
      o = zero16;
      for (int t = 0; t < 64; ++t) {
        const u32x4 gkl = *(const u32x4*)(kvh + t * 1024 + l * 16);
        const u32x2 gkh = *(const u32x2*)(kvh + KV_KHI + t * 256 + r32 * 8);
        const char* pv = kvh + KV_VT + t * VT_TILE + h * 304 + min(r32, 18) * 16;
        const u32x4 gv0 = *(const u32x4*)pv, gv1 = *(const u32x4*)(pv + 608);
        const f32x16 s = mfma32k8(gkh, qt, mfma32(gkl, ql, zero16));
        const float mn = fmaxf(m, rowmax(s)), al = fast_exp2(m - mn);
        m = mn;
#pragma unroll
        for (int i = 0; i < 16; ++i) o[i] *= al;
        u32x4 p0, p1;
#pragma unroll
        for (int j2 = 0; j2 < 4; ++j2) {
          p0[j2] = pk2(fast_exp2(s[2 * j2] - m), fast_exp2(s[2 * j2 + 1] - m));
          p1[j2] = pk2(fast_exp2(s[8 + 2 * j2] - m), fast_exp2(s[8 + 2 * j2 + 1] - m));
        }
        o = mfma32(gv0, p0, o);
        o = mfma32(gv1, p1, o);
      }
    }
  }
  auto lrow = __builtin_amdgcn_permlane32_swap(__float_as_uint(o[10]), __float_as_uint(o[10]), false, false);
  const float inv = 1.f / __uint_as_float(lrow[0]);
  const unsigned a0 = pk2(o[0] * inv, o[1] * inv), a1 = pk2(o[2] * inv, o[3] * inv);
  const unsigned b0 = pk2(o[4] * inv, o[5] * inv), b1 = pk2(o[6] * inv, o[7] * inv);
  const unsigned tail = pk2(o[8] * inv, o[9] * inv);
  auto r0 = __builtin_amdgcn_permlane32_swap(a0, b0, false, false);
  auto r1 = __builtin_amdgcn_permlane32_swap(a1, b1, false, false);
  const int tok = (tq + kh) * 32 + r32, mb = tok >> 4, row = tok & 15;
  char* ot = ws + WS_OT + (size_t)mb * 18432 + row * 16;
  const int bi = xg * 9 + 2 * hl + h, bt = xg * 9 + 8;
  *(u32x4*)(ot + (bi >> 2) * 1024 + (bi & 3) * 256) = u32x4{r0[0], r1[0], r0[1], r1[1]};
  if (h == 0) *(unsigned*)(ot + (bt >> 2) * 1024 + (bt & 3) * 256 + hl * 4) = tail;
  TS(2, 7);
}

constexpr int L_H = 0, L_X = 73728, L_STASH = 147456, L_GS = 156672, L_MLP = 161280;

constexpr int MLP_NS = 5;
template <int NB, int P0, int P1, int PEND, int POFF, bool BMAP>
DI void mlp_gemm(const char* __restrict__ wt, const char* A, int l, u32x4 (&wf)[MLP_NS][NB], f32x4 (&acc)[4][NB]) {
  constexpr int NS = MLP_NS;
  auto kmap = [](int p) { return BMAP ? (p < 8 ? p : (p < 10 ? p + 8 : p - 2)) : p; };
  u32x4 hf[2][4];
#pragma unroll
  for (int mb = 0; mb < 4; ++mb) hf[P0 & 1][mb] = *(const u32x4*)(A + mb * 18432 + kmap(P0) * 1024 + l * 16);
#pragma unroll
  for (int p = P0; p < P1; ++p) {
#pragma unroll
    for (int nb = 0; nb < NB; ++nb) {
      if (nb < 4 && p + 1 < P1) hf[(p + 1) & 1][nb] = *(const u32x4*)(A + nb * 18432 + kmap(p + 1) * 1024 + l * 16);
#pragma unroll
      for (int mb = 0; mb < 4; ++mb) acc[mb][nb] = mfma16(wf[(p + POFF) % NS][nb], hf[p & 1][mb], acc[mb][nb]);
      if (p + NS < PEND) wf[(p + POFF) % NS][nb] = *(const u32x4*)(wt + ((p + NS) * NB + nb) * 1024);
      SB();
    }
  }
}

template <int NB>
DI void mlp_body(char* __restrict__ ws, char* lds, const char* __restrict__ wt, u32x4 (&wf)[MLP_NS][NB], int xg, int r, int s, int l) {
  const int g = l >> 4, n = l & 15;
  f32x4 acc[4][NB];
#pragma unroll
  for (int mb = 0; mb < 4; ++mb)
#pragma unroll
    for (int nb = 0; nb < NB; ++nb) acc[mb][nb] = f32x4{0, 0, 0, 0};
  if (NB == 5) __builtin_amdgcn_s_setprio(2);
  mlp_gemm<NB, 0, 18, 36, 0, false>(wt, lds + L_H, l, wf, acc);
  TS(4, 5);
  if (NB == 4) asm volatile("s_barrier" ::: "memory");
  {
    char* X = lds + L_X;
    const int grp = NB == 5 ? s : 4 + s;
#pragma unroll
    for (int mb = 0; mb < 4; ++mb) {
      u32x2 xp[NB];
#pragma unroll
      for (int nb = 0; nb < NB; ++nb) {
        xp[nb] = u32x2{pk2(silu_f(acc[mb][nb][0]), silu_f(acc[mb][nb][1])), pk2(silu_f(acc[mb][nb][2]), silu_f(acc[mb][nb][3]))};
        acc[mb][nb] = f32x4{0, 0, 0, 0};
      }
#pragma unroll
      for (int p = 0; p < 2; ++p)
        *(u32x4*)(X + (mb * 18 + 2 * grp + p) * 1024 + l * 16) = u32x4{xp[2 * p][0], xp[2 * p][1], xp[2 * p + 1][0], xp[2 * p + 1][1]};
      if (NB == 5) *(u32x2*)(X + (mb * 18 + 16 + (s >> 1)) * 1024 + l * 16 + (s & 1) * 8) = xp[NB - 1];
    }
  }
  TS(4, 6);
  asm volatile("s_waitcnt lgkmcnt(0)\n\ts_barrier" ::: "memory");
  if (NB == 5) {
    mlp_gemm<NB, 0, 10, 18, 18, true>(wt + 18 * NB * 1024, lds + L_X, l, wf, acc);
    asm volatile("s_barrier" ::: "memory");
    mlp_gemm<NB, 10, 18, 18, 18, true>(wt + 18 * NB * 1024, lds + L_X, l, wf, acc);
  } else {
    mlp_gemm<NB, 0, 18, 18, 18, true>(wt + 18 * NB * 1024, lds + L_X, l, wf, acc);
  }
  TS(4, 7);
  const __amdgpu_buffer_rsrc_t rs = ws_rsrc(ws);
  const unsigned pbase = (unsigned)WS_PART + (unsigned)((xg * 32 + r) * 8) * 9216u;
  const int grp = NB == 5 ? s : 4 + s;
#pragma unroll
  for (int mb = 0; mb < 4; ++mb) {
#pragma unroll
    for (int q = 0; q < 4; q += 2) {
      const u32x2 h0 = u32x2{pk2(acc[mb][0][q], acc[mb][1][q]), pk2(acc[mb][2][q], acc[mb][3][q])};
      const u32x2 h1 = u32x2{pk2(acc[mb][0][q + 1], acc[mb][1][q + 1]), pk2(acc[mb][2][q + 1], acc[mb][3][q + 1])};
      const int ch = 8 * grp + 4 * (g & 1) + q + (g >> 1);
      st_dev(rs, pbase + (2 * mb + (n >> 3)) * 9216 + ch * 128 + (n & 7) * 16, join32(h0, h1));
    }
  }
  if (NB == 5) {
#pragma unroll
    for (int mb = 0; mb < 4; mb += 2) {
      const u32x2 h0 = u32x2{pk2(acc[mb][NB - 1][0], acc[mb][NB - 1][1]), pk2(acc[mb][NB - 1][2], acc[mb][NB - 1][3])};
      const u32x2 h1 = u32x2{pk2(acc[mb + 1][NB - 1][0], acc[mb + 1][NB - 1][1]), pk2(acc[mb + 1][NB - 1][2], acc[mb + 1][NB - 1][3])};
      const int ch = 64 + 2 * s + (g >> 1);
      st_dev(rs, pbase + (2 * (mb + (g & 1)) + (n >> 3)) * 9216 + ch * 128 + (n & 7) * 16, join16(h0, h1));
    }
    __builtin_amdgcn_s_setprio(0);
  }
}

template <int NB>
DI void mlp_all(const u16* __restrict__ x, char* __restrict__ ws, u16* __restrict__ out, char* lds, const int w) {
  const int b = blockIdx.x, tid = threadIdx.x, l = tid & 63;
  const int xg = (b & 6) + ((b >> 3) & 1), r = 2 * (b >> 4) + (b & 1), s = w & 3;
  const int g = l >> 4, n = l & 15;
  gu32* cnt1 = (gu32*)(ws + WS_FLAG) + r * 64;
  gu32* cnt2 = (gu32*)(ws + WS_FLAG) + (32 + r) * 64;
  gu32* tmo = (gu32*)(ws + WS_FLAG) + 64 * 64;
  const __amdgpu_buffer_rsrc_t rs = ws_rsrc(ws);
  TS(4, 0);
  const char* wt = ws + WS_W12 + (size_t)xg * 1296 * 1024 + (size_t)(NB == 5 ? s * 180 : 720 + s * 144) * 1024 + l * 16;
  u32x4 wf[MLP_NS][NB];
  const int rh = (xg >> 1) & 1, cq = 2 * (xg >> 2) + (xg & 1);
  const float* mod = (const float*)(ws + WS_MOD);
  {
    char* O = lds + L_X;
    const char* src = ws + WS_OT + (size_t)(4 * r + 2 * rh) * 18432 + l * 16;
#pragma unroll
    for (int i = 0; i < 5; ++i) {
      const int pc = min(w + 8 * i, 35);
      glds16(src + pc * 1024, (unsigned)__builtin_amdgcn_readfirstlane((unsigned)(uintptr_t)O + pc * 1024));
    }
    u32x4 wo[18], wo8[3];
    const char* wot = ws + WS_WO + (size_t)(cq * 9 * 18) * 1024 + l * 16;
#pragma unroll
    for (int ks = 0; ks < 18; ++ks) wo[ks] = *(const u32x4*)(wot + (w * 18 + ks) * 1024);
#pragma unroll
    for (int i = 0; i < 3; ++i) wo8[i] = *(const u32x4*)(wot + (8 * 18 + min(w + 8 * i, 17)) * 1024);
    u32x2 xr[2];
#pragma unroll
    for (int mb = 0; mb < 2; ++mb) xr[mb] = *(const u32x2*)(x + (size_t)(r * 64 + rh * 32 + mb * 16 + n) * D_ + cq * 144 + w * 16 + 4 * g);
    const f32x4 gav = *(const f32x4*)(mod + 2 * 576 + cq * 144 + w * 16 + 4 * g);
    u32x2 xr8[2]; f32x4 gav8;
    if (NB == 5 && w == 0) {
#pragma unroll
      for (int mb = 0; mb < 2; ++mb) xr8[mb] = *(const u32x2*)(x + (size_t)(r * 64 + rh * 32 + mb * 16 + n) * D_ + cq * 144 + 128 + 4 * g);
      gav8 = *(const f32x4*)(mod + 2 * 576 + cq * 144 + 128 + 4 * g);
    }
    f32x4 gv = {0, 0, 0, 0};
    if (tid < 288) gv = *(const f32x4*)(mod + (tid < 144 ? 4 * 576 + tid * 4 : 3 * 576 + tid * 4 - 576));
    asm volatile("s_barrier" ::: "memory");
#pragma unroll
    for (int p = 0; p < MLP_NS; ++p)
#pragma unroll
      for (int nb = 0; nb < NB; ++nb) wf[p][nb] = *(const u32x4*)(wt + (p * NB + nb) * 1024);
    SB();
    asm volatile("" : "+v"(wo[0]));
    if (tid < 288) *(f32x4*)(lds + L_GS + tid * 16) = gv;
    __syncthreads();
    TS(4, 1);
    f32x4 acc[2] = {{0, 0, 0, 0}, {0, 0, 0, 0}}, a8[2] = {{0, 0, 0, 0}, {0, 0, 0, 0}};
#pragma unroll
    for (int i = 0; i < 3; ++i) {
      const int ks = w + 8 * i;
      if (i < 2 || w < 2) {
#pragma unroll
        for (int mb = 0; mb < 2; ++mb) a8[mb] = mfma16(wo8[i], *(const u32x4*)(O + (mb * 18 + ks) * 1024 + l * 16), a8[mb]);
      }
    }
#pragma unroll
    for (int mb = 0; mb < 2; ++mb) *(f32x4*)(O + 36864 + (w * 2 + mb) * 1024 + l * 16) = a8[mb];
#pragma unroll
    for (int ks = 0; ks < 18; ++ks)
#pragma unroll
      for (int mb = 0; mb < 2; ++mb) acc[mb] = mfma16(wo[ks], *(const u32x4*)(O + (mb * 18 + ks) * 1024 + l * 16), acc[mb]);
    auto x1pk = [&](const f32x4& a, const u32x2& xv, const f32x4& gg) {
      return u32x2{pk2(lo2f(xv[0]) + gg[0] * a[0], hi2f(xv[0]) + gg[1] * a[1]), pk2(lo2f(xv[1]) + gg[2] * a[2], hi2f(xv[1]) + gg[3] * a[3])};
    };
    const unsigned x1b = (unsigned)WS_X1 + r * 73728u + (2 * rh + (g & 1)) * 18432u + n * 16;
    st_dev(rs, x1b + (cq * 18 + 2 * w + (g >> 1)) * 256, join16(x1pk(acc[0], xr[0], gav), x1pk(acc[1], xr[1], gav)));
    __syncthreads();
    if (NB == 5 && w == 0) {
#pragma unroll
      for (int mb = 0; mb < 2; ++mb) {
        a8[mb] = f32x4{0, 0, 0, 0};
#pragma unroll
        for (int i = 0; i < 8; ++i) a8[mb] += *(const f32x4*)(O + 36864 + (i * 2 + mb) * 1024 + l * 16);
      }
      st_dev(rs, x1b + (cq * 18 + 16 + (g >> 1)) * 256, join16(x1pk(a8[0], xr8[0], gav8), x1pk(a8[1], xr8[1], gav8)));
    }
  }
  TS(4, 2);
  publish(cnt1);
  await(cnt1, 8, tmo);
  TS(4, 3);
  {
    u32x4 xv[9];
    const unsigned src = (unsigned)WS_X1 + r * 73728u + (w >> 1) * 18432u + ((w & 1) * 8 + (l & 7)) * 16 + (l >> 3) * 256;
#pragma unroll
    for (int i = 0; i < 9; ++i) xv[i] = ld_dev(rs, src + i * 2048);
    SB();
    float sm = 0.f, sq = 0.f;
#pragma unroll
    for (int i = 0; i < 9; ++i)
#pragma unroll
      for (int j = 0; j < 4; ++j) { sm = dot2(xv[i][j], 0x3f803f80u, sm); sq = dot2(xv[i][j], xv[i][j], sq); }
    if (w == xg) {
#pragma unroll
      for (int i = 0; i < 9; ++i) *(u32x4*)(lds + L_STASH + ((l >> 3) + 8 * i) * 128 + (l & 7) * 16) = xv[i];
    }
    const float mu = red_sub(sm) * (1.f / D_);
    const float var = fmaxf(red_sub(sq) * (1.f / D_) - mu * mu, 0.f);
    const float rstd = 1.f / sqrtf(var + LN_EPS);
    ln_apply(xv, f32x2{rstd, -mu * rstd}, (const float*)(lds + L_GS), lds + L_H, w, l);
  }
  __syncthreads();
  TS(4, 4);
  mlp_body<NB>(ws, lds, wt, wf, xg, r, s, l);
  TS(4, 8);
  publish(cnt2);
  f32x4 gbv[2][2];
  if (tid < 288) {
#pragma unroll
    for (int c = 0; c < 2; ++c) {
      gbv[c][0] = *(const f32x4*)(mod + 5 * 576 + ((tid + 288 * c) >> 3) * 8);
      gbv[c][1] = *(const f32x4*)(mod + 5 * 576 + ((tid + 288 * c) >> 3) * 8 + 4);
    }
  }
  await(cnt2, 8, tmo);
  TS(4, 9);
  if (tid < 288) {
    u32x4 pp[2][8];
    const unsigned pb = (unsigned)WS_PART + (unsigned)(r * 8 + xg) * 9216u + tid * 16;
#pragma unroll
    for (int c = 0; c < 2; ++c)
#pragma unroll
      for (int sl = 0; sl < 8; ++sl) pp[c][sl] = ld_dev(rs, pb + sl * (32 * 8 * 9216u) + c * 4608);
    SB();
#pragma unroll
    for (int c = 0; c < 2; ++c) {
      const int idx = tid + 288 * c;
      const u32x4 xv = *(const u32x4*)(lds + L_STASH + idx * 16);
      u32x4 o;
#pragma unroll
      for (int j = 0; j < 4; ++j) {
        float a = 0.f, cc = 0.f;
#pragma unroll
        for (int sl = 0; sl < 8; ++sl) { a += lo2f(pp[c][sl][j]); cc += hi2f(pp[c][sl][j]); }
        o[j] = pk2(lo2f(xv[j]) + gbv[c][j >> 1][(2 * j) & 3] * a, hi2f(xv[j]) + gbv[c][j >> 1][(2 * j + 1) & 3] * cc);
      }
      *(u32x4*)(out + (size_t)(r * 64 + xg * 8 + (idx & 7)) * D_ + (idx >> 3) * 8) = o;
    }
  }
  TS(4, 10);
}

__global__ __launch_bounds__(512, 2) void k_mlp(const u16* __restrict__ x, char* __restrict__ ws, u16* __restrict__ out) {
  extern __shared__ __attribute__((aligned(16))) char lds[];
  const int w = __builtin_amdgcn_readfirstlane(threadIdx.x >> 6);
  if (w < 4) mlp_all<5>(x, ws, out, lds, w);
  else mlp_all<4>(x, ws, out, lds, w);
}

extern "C" void kernel_launch(void* const* d_in, const int* in_sizes, int n_in, void* d_out, int out_size, void* d_ws, size_t ws_size,
                              hipStream_t stream) {
  const u16* x = (const u16*)d_in[0];
  const u16* wqkv = (const u16*)d_in[1];
  const u16* wo = (const u16*)d_in[2];
  const u16* w1 = (const u16*)d_in[4];
  const u16* w2 = (const u16*)d_in[5];
  const u16* c = (const u16*)d_in[6];
  const u16* wmod = (const u16*)d_in[7];
  char* ws = (char*)d_ws;
  (void)hipFuncSetAttribute((const void*)k_attn, hipFuncAttributeMaxDynamicSharedMemorySize, 163840);
  (void)hipFuncSetAttribute((const void*)k_mlp, hipFuncAttributeMaxDynamicSharedMemorySize, L_MLP);
  k_prep<<<256, 448, 0, stream>>>(x, wqkv, c, wmod, ws);
  k_qkv<<<256, 512, 0, stream>>>(x, ws);
  k_attn<<<256, 1024, 163840, stream>>>(wo, w1, w2, ws);
  k_mlp<<<256, 512, L_MLP, stream>>>(x, ws, (u16*)d_out);
}
```

```cpp
#include <hip/hip_runtime.h>
#include <hip/hip_bf16.h>
#include <stdint.h>
#include <type_traits>

typedef __attribute__((ext_vector_type(8))) __bf16 bf16x8;
typedef __attribute__((ext_vector_type(2))) __bf16 bf16x2;
typedef __attribute__((ext_vector_type(4))) short s16x4;
typedef __attribute__((ext_vector_type(2))) float f32x2;
typedef __attribute__((ext_vector_type(4))) float f32x4;
typedef __attribute__((ext_vector_type(16))) float f32x16;
typedef __attribute__((ext_vector_type(2))) unsigned u32x2;
typedef __attribute__((ext_vector_type(4))) unsigned u32x4;
typedef unsigned short u16;
typedef __attribute__((address_space(1))) unsigned gu32;

#define DI __device__ __forceinline__
#define SB() __builtin_amdgcn_sched_barrier(0)

constexpr int S_ = 2048, D_ = 576, F_ = 4608;
constexpr float LN_EPS = 1e-5f;
constexpr float QSCALE = 0.23570226039551584f * 1.4426950408889634f;

constexpr size_t WS_FLAG = 0;
constexpr size_t WS_MOD  = 20480;
constexpr size_t WS_STAT = 36864;
constexpr size_t WS_WQKV = 53248;
constexpr size_t WS_QLO  = WS_WQKV + 2064384;
constexpr size_t WS_QHI  = WS_QLO + 2097152;
constexpr int    KV_HEAD = 159744, KV_KHI = 65536, KV_VT = 81920, VT_TILE = 1216;
constexpr size_t WS_KV   = WS_QHI + 262144;
constexpr size_t WS_OT   = WS_KV + 32 * (size_t)KV_HEAD;
constexpr size_t WS_WO   = WS_OT + 2359296;
constexpr size_t WS_W12  = WS_WO + 663552;
constexpr size_t WS_X1   = WS_W12 + 2 * 5308416;
constexpr size_t WS_PART = WS_X1 + 2359296;
constexpr size_t WS_END  = WS_PART + 18874368;

#define TS(K, I)

DI float bf2f(u16 v) { return __uint_as_float((unsigned)v << 16); }
DI float lo2f(unsigned v) { return __uint_as_float(v << 16); }
DI float hi2f(unsigned v) { return __uint_as_float(v & 0xffff0000u); }
DI unsigned pk2(float a, float b) { bf16x2 v = {(__bf16)a, (__bf16)b}; return __builtin_bit_cast(unsigned, v); }
DI float rbf(float a) { return (float)(__bf16)a; }
DI f32x4 mfma16(u32x4 a, u32x4 b, f32x4 c) {
  return __builtin_amdgcn_mfma_f32_16x16x32_bf16(__builtin_bit_cast(bf16x8, a), __builtin_bit_cast(bf16x8, b), c, 0, 0, 0);
}
DI f32x16 mfma32(u32x4 a, u32x4 b, f32x16 c) {
  return __builtin_amdgcn_mfma_f32_32x32x16_bf16(__builtin_bit_cast(bf16x8, a), __builtin_bit_cast(bf16x8, b), c, 0, 0, 0);
}
DI f32x16 mfma32k8(u32x2 a, u32x2 b, f32x16 c) {
  return __builtin_amdgcn_mfma_f32_32x32x8bf16_1k(__builtin_bit_cast(s16x4, a), __builtin_bit_cast(s16x4, b), c, 0, 0, 0);
}
DI float fast_rcp(float x) { return __builtin_amdgcn_rcpf(x); }
DI float fast_exp2(float x) { return __builtin_amdgcn_exp2f(x); }
DI float silu_f(float x) { return x * fast_rcp(1.f + fast_exp2(-1.4426950408889634f * x)); }
DI float xor_add(float v, int m) { return v + __shfl_xor(v, m); }
DI float dot2(unsigned a, unsigned b, float c) {
  return __builtin_amdgcn_fdot2_f32_bf16(__builtin_bit_cast(bf16x2, a), __builtin_bit_cast(bf16x2, b), c, false);
}
DI float red_sub(float v) {
  v += __builtin_bit_cast(float, __builtin_amdgcn_update_dpp(0, __builtin_bit_cast(int, v), 0x128, 0xf, 0xf, false));
  auto a = __builtin_amdgcn_permlane16_swap(__float_as_uint(v), __float_as_uint(v), false, false);
  v = __uint_as_float(a[0]) + __uint_as_float(a[1]);
  auto b = __builtin_amdgcn_permlane32_swap(__float_as_uint(v), __float_as_uint(v), false, false);
  return __uint_as_float(b[0]) + __uint_as_float(b[1]);
}
DI u32x4 join16(u32x2 x, u32x2 y) {
  auto a = __builtin_amdgcn_permlane16_swap(x[0], y[0], false, false);
  auto b = __builtin_amdgcn_permlane16_swap(x[1], y[1], false, false);
  return u32x4{a[0], b[0], a[1], b[1]};
}
DI u32x4 join32(u32x2 x, u32x2 y) {
  auto a = __builtin_amdgcn_permlane32_swap(x[0], y[0], false, false);
  auto b = __builtin_amdgcn_permlane32_swap(x[1], y[1], false, false);
  return u32x4{a[0], b[0], a[1], b[1]};
}
DI void glds16(const void* g, unsigned lds_base) {
  unsigned sv;
  asm volatile("s_mov_b32 %0, m0\n\ts_mov_b32 m0, %2\n\ts_nop 0\n\tglobal_load_lds_dwordx4 %1, off\n\ts_mov_b32 m0, %0"
               : "=&s"(sv) : "v"(g), "s"(lds_base) : "memory");
}
DI __amdgpu_buffer_rsrc_t ws_rsrc(char* ws) { return __builtin_amdgcn_make_buffer_rsrc(ws, 0, (int)WS_END, 0x00020000); }
DI void st_dev(__amdgpu_buffer_rsrc_t rs, unsigned off, u32x4 v) { __builtin_amdgcn_raw_buffer_store_b128(v, rs, off, 0, 16); }
DI u32x4 ld_dev(__amdgpu_buffer_rsrc_t rs, unsigned off) { return __builtin_amdgcn_raw_buffer_load_b128(rs, off, 0, 16); }
DI void publish(gu32* cnt) {
  asm volatile("s_waitcnt vmcnt(0)" ::: "memory");
  __syncthreads();
  if (threadIdx.x == 0) __hip_atomic_fetch_add(cnt, 1u, __ATOMIC_RELAXED, __HIP_MEMORY_SCOPE_AGENT);
}
DI void await(gu32* cnt, unsigned want, gu32* tmo) {
  if (threadIdx.x == 0) {
    for (unsigned spins = 0; __hip_atomic_load(cnt, __ATOMIC_RELAXED, __HIP_MEMORY_SCOPE_AGENT) < want;) {
      __builtin_amdgcn_s_sleep(1);
      if (++spins > (1u << 20)) { __hip_atomic_store(tmo, 1u, __ATOMIC_RELAXED, __HIP_MEMORY_SCOPE_AGENT); break; }
    }
  }
  __builtin_amdgcn_fence(__ATOMIC_ACQUIRE, "wavefront");
  __syncthreads();
}

struct Unit { const u16* src; char* p; int ld, a, b, c, s1, s2; };
DI void unit_load(const Unit& u, u32x4 (&in)[8]) {
#pragma unroll
  for (int j = 0; j < 8; ++j) in[j] = *(const u32x4*)(u.src + (size_t)(u.a * (j >> 2) + u.b * ((j >> 1) & 1) + u.c * (j & 1)) * u.ld);
}
DI u32x4 unit_col(const u32x4 (&in)[8], int i) {
  u32x4 o;
#pragma unroll
  for (int m = 0; m < 4; ++m) o[m] = __builtin_amdgcn_perm(in[2 * m + 1][i >> 1], in[2 * m][i >> 1], (i & 1) ? 0x07060302u : 0x05040100u);
  return o;
}

__global__ __launch_bounds__(448) void k_prep(const u16* __restrict__ x, const u16* __restrict__ wqkv, const u16* __restrict__ c,
                                              const u16* __restrict__ wmod, char* __restrict__ ws) {
  __shared__ float red[128 * 16];
  const int b = blockIdx.x, tid = threadIdx.x, l = tid & 63;
  const int w = __builtin_amdgcn_readfirstlane(tid >> 6);
  TS(0, 0);
  if (w < 4) {
    if (b >= 216) {
      if (b == 255) for (int i = tid; i < 5120; i += 256) ((unsigned*)(ws + WS_FLAG))[i] = 0u;
      return;
    }
    const int cg = (b & 7) * 27 + (b >> 3);
    const int half = tid & 1, row0 = tid >> 1;
    u32x4 wv[5]; u16 cv[5];
#pragma unroll
    for (int i = 0; i < 5; ++i) {
      const int k = min(row0 + 128 * i, 575);
      wv[i] = *(const u32x4*)(wmod + (size_t)k * 3456 + cg * 16 + half * 8);
      cv[i] = c[k];
    }
    float acc[8];
#pragma unroll
    for (int j = 0; j < 8; ++j) acc[j] = 0.f;
#pragma unroll
    for (int i = 0; i < 5; ++i) {
      float s = rbf(silu_f(bf2f(cv[i])));
      if (row0 + 128 * i >= 576) s = 0.f;
#pragma unroll
      for (int j = 0; j < 4; ++j) { acc[2 * j] += s * lo2f(wv[i][j]); acc[2 * j + 1] += s * hi2f(wv[i][j]); }
    }
    *(f32x4*)&red[row0 * 16 + half * 8] = f32x4{acc[0], acc[1], acc[2], acc[3]};
    *(f32x4*)&red[row0 * 16 + half * 8 + 4] = f32x4{acc[4], acc[5], acc[6], acc[7]};
    __syncthreads();
    if (w == 0) {
      const int col = l & 15, part = l >> 4;
      float s = 0.f;
#pragma unroll
      for (int i = 0; i < 32; ++i) s += red[(part * 32 + i) * 16 + col];
      s = xor_add(s, 16); s = xor_add(s, 32);
      if (l < 16) {
        const int n = cg * 16 + col, sec = n / 576;
        float v = rbf(s);
        if (sec == 1 || sec == 4) v = rbf(1.f + v);
        ((float*)(ws + WS_MOD))[n] = v;
      }
    }
    TS(0, 1);
  } else if (w == 4) {
    const int row = 8 * b + (l & 7), sub = l >> 3;
    u32x4 xv[9];
#pragma unroll
    for (int i = 0; i < 9; ++i) xv[i] = *(const u32x4*)(x + (size_t)row * D_ + (sub + 8 * i) * 8);
    float s = 0.f;
#pragma unroll
    for (int i = 0; i < 9; ++i)
#pragma unroll
      for (int j = 0; j < 4; ++j) s += lo2f(xv[i][j]) + hi2f(xv[i][j]);
    const float mu = red_sub(s) * (1.f / D_);
    float q = 0.f;
#pragma unroll
    for (int i = 0; i < 9; ++i)
#pragma unroll
      for (int j = 0; j < 4; ++j) { const float a = lo2f(xv[i][j]) - mu, bb = hi2f(xv[i][j]) - mu; q += a * a + bb * bb; }
    q = red_sub(q);
    const float rstd = 1.f / sqrtf(q * (1.f / D_) + LN_EPS);
    if (sub == 0) ((f32x2*)(ws + WS_STAT))[row] = f32x2{rstd, -mu * rstd};
    TS(0, 1);
  } else {
    const int xg = b & 7, rank = b >> 3;
    char* dst = ws + WS_WQKV + (size_t)xg * 252 * 1024;
    if (w == 5) {
      if (l < 54) {
        const int i = rank * 54 + l, kg = i / 24, rem = i % 24, part = rem >> 3, hl = (rem >> 1) & 3, half = rem & 1;
        Unit u;
        u.src = wqkv + (size_t)kg * 8 * 1728 + part * 576 + (4 * xg + hl) * 18 + half * 8;
        u.ld = 1728; u.a = 4; u.b = 2; u.c = 1;
        char* p = dst + ((kg >> 2) * 14 + (part < 2 ? 2 * hl + part : 8 + hl)) * 1024 + ((kg & 3) * 16 + half * 8) * 16;
        u32x4 in[8];
        unit_load(u, in);
#pragma unroll
        for (int i2 = 0; i2 < 8; ++i2) *(u32x4*)(p + i2 * 16) = unit_col(in, i2);
      }
    } else {
      if (l < 9) {
        const int i = rank * 9 + l, kg = i >> 2, hl = i & 3;
        const u16* src = wqkv + (size_t)kg * 8 * 1728 + (4 * xg + hl) * 18 + 16;
        unsigned in[3][8];
#pragma unroll
        for (int p = 0; p < 3; ++p)
#pragma unroll
          for (int j = 0; j < 8; ++j) in[p][j] = *(const unsigned*)(src + (size_t)j * 1728 + p * 576);
        char* pq = dst + ((kg >> 2) * 14 + 12) * 1024 + (kg & 3) * 256;
#pragma unroll
        for (int p = 0; p < 3; ++p)
#pragma unroll
          for (int e = 0; e < 2; ++e) {
            u32x4 o;
#pragma unroll
            for (int m = 0; m < 4; ++m) o[m] = __builtin_amdgcn_perm(in[p][2 * m + 1], in[p][2 * m], e ? 0x07060302u : 0x05040100u);
            if (p < 2) *(u32x4*)(pq + (4 * hl + 2 * p + e) * 16) = o;
            else { *(u32x4*)(pq + 1024 + (2 * hl + e) * 16) = o; *(u32x4*)(pq + 1024 + (8 + 2 * hl + e) * 16) = u32x4{0, 0, 0, 0}; }
          }
      }
    }
    TS(0, 1);
  }
}

DI void ln_apply(const u32x4 (&xv)[9], f32x2 st, const float* gs, char* A, int w, int l) {
  const int sub = l >> 3;
  f32x4 gq[2][4];
  auto fetch = [&](int i, f32x4 (&q)[4]) {
    const float* p = gs + (sub + 8 * i) * 8;
    q[0] = *(const f32x4*)p; q[1] = *(const f32x4*)(p + 4); q[2] = *(const f32x4*)(p + 576); q[3] = *(const f32x4*)(p + 580);
  };
  fetch(0, gq[0]);
#pragma unroll
  for (int i = 0; i < 9; ++i) {
    if (i + 1 < 9) fetch(i + 1, gq[(i + 1) & 1]);
    SB();
    const f32x4 (&q)[4] = gq[i & 1];
    u32x4 o;
#pragma unroll
    for (int j = 0; j < 4; ++j) {
      const float ga = q[j >> 1][(2 * j) & 3], gb = q[j >> 1][(2 * j + 1) & 3];
      const float sa = q[2 + (j >> 1)][(2 * j) & 3], sb = q[2 + (j >> 1)][(2 * j + 1) & 3];
      const float a = (lo2f(xv[i][j]) * st[0] + st[1]) * ga + sa;
      const float bb = (hi2f(xv[i][j]) * st[0] + st[1]) * gb + sb;
      o[j] = pk2(a, bb);
    }
    *(u32x4*)(A + (w >> 1) * 18432 + (sub + 8 * i) * 256 + ((w & 1) * 8 + (l & 7)) * 16) = o;
    SB();
  }
}

template <bool TFORM>
DI void qkv_gemm(const char* __restrict__ wt, const char* A, int s, int l, f32x4 (&a0)[4], f32x4 (&a1)[4], f32x4& at,
                 u32x4 (&wf)[6][3]) {
  constexpr int NS = 6;
  u32x4 hf[2][4];
#pragma unroll
  for (int i = 0; i < 4; ++i) hf[0][i] = *(const u32x4*)(A + ((i + s) & 3) * 18432 + l * 16);
#pragma unroll
  for (int ks = 0; ks < 18; ++ks) {
    const int kk = ks % NS;
#pragma unroll
    for (int i = 0; i < 4; ++i) {
      if (ks + 1 < 18) hf[(ks + 1) & 1][i] = *(const u32x4*)(A + ((i + s) & 3) * 18432 + (ks + 1) * 1024 + l * 16);
      if (TFORM) {
        a0[i] = mfma16(wf[kk][0], hf[ks & 1][i], a0[i]);
        a1[i] = mfma16(wf[kk][1], hf[ks & 1][i], a1[i]);
        if (i == 0) at = mfma16(wf[kk][2], hf[ks & 1][0], at);
      } else {
        a0[i] = mfma16(hf[ks & 1][i], wf[kk][0], a0[i]);
        if (i == 0) at = mfma16(hf[ks & 1][0], wf[kk][2], at);
      }
      SB();
    }
    if (ks + NS < 18) {
      wf[kk][0] = *(const u32x4*)(wt + (ks + NS) * 14336);
      if (TFORM) wf[kk][1] = *(const u32x4*)(wt + (ks + NS) * 14336 + 1024);
      wf[kk][2] = *(const u32x4*)(wt + (ks + NS) * 14336 + (TFORM ? 12 - 2 * s : 13 - 8 - s) * 1024);
    }
    SB();
  }
}

__global__ __launch_bounds__(512, 2) void k_qkv(const u16* __restrict__ x, char* __restrict__ ws) {
  __shared__ __attribute__((aligned(16))) char A[73728];
  __shared__ __attribute__((aligned(16))) float gs[1152];
  const int b = blockIdx.x, tid = threadIdx.x, l = tid & 63;
  const int w = __builtin_amdgcn_readfirstlane(tid >> 6);
  const int xg = (b & 6) + ((b >> 3) & 1), r = 2 * (b >> 4) + (b & 1), s = w & 3;
  TS(1, 0);
  u32x4 xv[9];
  const int row = r * 64 + 8 * w + (l & 7);
#pragma unroll
  for (int i = 0; i < 9; ++i) xv[i] = *(const u32x4*)(x + (size_t)row * D_ + ((l >> 3) + 8 * i) * 8);
  const f32x2 st = ((const f32x2*)(ws + WS_STAT))[row];
  const int gi = min(tid, 287);
  const f32x4 gv = *(const f32x4*)((const float*)(ws + WS_MOD) + (gi < 144 ? 576 + gi * 4 : gi * 4 - 576));
  asm volatile("s_barrier" ::: "memory");
  const char* wt = ws + WS_WQKV + (size_t)xg * 252 * 1024 + (w < 4 ? 2 * s : 8 + s) * 1024 + l * 16;
  u32x4 wf[6][3];
#pragma unroll
  for (int ks = 0; ks < 6; ++ks) {
    wf[ks][0] = *(const u32x4*)(wt + ks * 14336);
    if (w < 4) wf[ks][1] = *(const u32x4*)(wt + ks * 14336 + 1024);
    wf[ks][2] = *(const u32x4*)(wt + ks * 14336 + (w < 4 ? 12 - 2 * s : 13 - 8 - s) * 1024);
  }
  SB();
  if (tid < 288) *(f32x4*)(gs + tid * 4) = gv;
  __syncthreads();
  TS(1, 7);
  ln_apply(xv, st, gs, A, w, l);
  TS(1, 1);
  __syncthreads();
  TS(1, 2);
  f32x4 a0[4], a1[4], at = {0, 0, 0, 0};
#pragma unroll
  for (int i = 0; i < 4; ++i) { a0[i] = at; a1[i] = at; }
  const int g = l >> 4, n = l & 15;
  const int head = 4 * xg + s;
  char* kvh = ws + WS_KV + (size_t)head * KV_HEAD;
  if (w < 4) {
    qkv_gemm<true>(wt, A, s, l, a0, a1, at, wf);
    TS(1, 3);
    char* qlo = ws + WS_QLO + (size_t)head * 65536;
#pragma unroll
    for (int i = 0; i < 4; i += 2) {
      const int mb = (i + (g & 1) + s) & 3;
      const int off = (2 * r + (mb >> 1)) * 1024 + (g >> 1) * 512 + (16 * (mb & 1) + n) * 16;
      u32x2 q0, q1, k0, k1;
      q0 = u32x2{pk2(a0[i][0] * QSCALE, a0[i][1] * QSCALE), pk2(a0[i][2] * QSCALE, a0[i][3] * QSCALE)};
      q1 = u32x2{pk2(a0[i + 1][0] * QSCALE, a0[i + 1][1] * QSCALE), pk2(a0[i + 1][2] * QSCALE, a0[i + 1][3] * QSCALE)};
      k0 = u32x2{pk2(a1[i][0], a1[i][1]), pk2(a1[i][2], a1[i][3])};
      k1 = u32x2{pk2(a1[i + 1][0], a1[i + 1][1]), pk2(a1[i + 1][2], a1[i + 1][3])};
      *(u32x4*)(qlo + off) = join16(q0, q1);
      *(u32x4*)(kvh + off) = join16(k0, k1);
    }
    const int tok = r * 64 + s * 16 + n, hg = 4 * xg + g;
    ((unsigned*)(ws + WS_QHI))[hg * 2048 + tok] = pk2(at[0] * QSCALE, at[1] * QSCALE);
    *(u32x2*)(ws + WS_KV + (size_t)hg * KV_HEAD + KV_KHI + tok * 8) = u32x2{pk2(at[2], at[3]), 0x00003f80u};
    __builtin_amdgcn_s_waitcnt(0);
  } else {
    qkv_gemm<false>(wt, A, s, l, a0, a1, at, wf);
    TS(1, 3);
#pragma unroll
    for (int i = 0; i < 4; i += 2) {
      const int mb = (i + (g >> 1) + s) & 3;
      const u32x2 v0 = u32x2{pk2(a0[i][0], a0[i][1]), pk2(a0[i][2], a0[i][3])};
      const u32x2 v1 = u32x2{pk2(a0[i + 1][0], a0[i + 1][1]), pk2(a0[i + 1][2], a0[i + 1][3])};
      *(u32x4*)(kvh + KV_VT + (2 * r + (mb >> 1)) * VT_TILE + (mb & 1) * 608 + (g & 1) * 304 + n * 16) = join32(v0, v1);
    }
    if (n < 12) {
      const int hg = 4 * xg + (n < 8 ? n >> 1 : n - 8), vr = n < 8 ? 16 + (n & 1) : 18;
      const u32x2 v = n < 8 ? u32x2{pk2(at[0], at[1]), pk2(at[2], at[3])} : u32x2{0x3f803f80u, 0x3f803f80u};
      *(u32x2*)(ws + WS_KV + (size_t)hg * KV_HEAD + KV_VT + (2 * r + (s >> 1)) * VT_TILE + (s & 1) * 608 + (g & 1) * 304 + vr * 16 + (g >> 1) * 8) = v;
    }
  }
  TS(1, 4);
}

DI size_t w12_frag(int grp, int j, int step) {
  return grp < 4 ? grp * 180 + step * 5 + j : (grp < 8 ? 720 + (grp - 4) * 144 + step * 4 + j : j * 180 + step * 5 + 4);
}
DI Unit retile_unit(int u0, int xg, int l, const u16* __restrict__ wo, const u16* __restrict__ w1, const u16* __restrict__ w2, char* __restrict__ ws) {
  Unit u;
  const int cl = l & 7, kgl = l >> 3;
  char* w12 = ws + WS_W12 + (size_t)xg * 1296 * 1024;
  if (u0 < 81) {
    const int grp = u0 / 9, kg = (u0 % 9) * 8 + kgl, ks = kg >> 2, kq = kg & 3;
    u.src = w1 + (size_t)kg * 8 * F_ + xg * 576 + grp * 64 + cl * 8; u.ld = F_; u.a = 4; u.b = 2; u.c = 1;
    u.p = w12 + w12_frag(grp, 0, ks) * 1024 + kq * 256 + cl * 16;
    u.s1 = grp < 8 ? 1024 : 180 * 1024; u.s2 = 128;
  } else if (u0 < 162) {
    const int u2 = u0 - 81, grp = u2 / 9, kg = (u2 % 9) * 8 + kgl, ks = kg >> 2, kq = kg & 3;
    const int row = (ks >> 1) * 64 + 32 * (kq & 1) + 4 * (kq >> 1) + 2 * (ks & 1);
    const int step = 18 + (ks < 8 ? ks : (ks < 16 ? ks + 2 : ks - 8));
    u.src = w2 + (size_t)(xg * 576 + row) * D_ + grp * 64 + cl * 8; u.ld = D_; u.a = 1; u.b = 16; u.c = 8;
    if (grp < 8) { u.p = w12 + w12_frag(grp, 0, step) * 1024 + kq * 256 + cl * 16; u.s1 = 1024; u.s2 = 128; }
    else { u.p = w12 + w12_frag(8, cl >> 1, step) * 1024 + kq * 256 + (cl & 1) * 128; u.s1 = 16; u.s2 = 64; }
  } else {
    const int i3 = min(xg * 648 + (u0 - 162) * 64 + l, 5183), kg = i3 / 72, cg = i3 % 72, ks = kg >> 2, kq = kg & 3;
    const int xx = kg / 9, uu = kg % 9;
    const int row = uu < 8 ? (4 * xx + (uu >> 1)) * 18 + 8 * (uu & 1) : 4 * xx * 18 + 16;
    u.src = wo + (size_t)row * D_ + cg * 8; u.ld = D_; u.a = uu < 8 ? 4 : 36; u.b = uu < 8 ? 2 : 18; u.c = 1;
    u.p = ws + WS_WO + (size_t)((cg >> 1) * 18 + ks) * 1024 + (kq * 16 + (cg & 1) * 8) * 16;
    u.s1 = 16; u.s2 = 64;
  }
  return u;
}

constexpr int L_ZERO = 160768;
typedef __attribute__((address_space(3))) u32x4 lds_u32x4;
typedef __attribute__((address_space(3))) u32x2 lds_u32x2;
DI u32x4 ldsr4(unsigned a) { return *(const lds_u32x4*)(uintptr_t)a; }
DI u32x2 ldsr2(unsigned a) { return *(const lds_u32x2*)(uintptr_t)a; }

__global__ __launch_bounds__(1024, 4) void k_attn(const u16* __restrict__ wo, const u16* __restrict__ w1, const u16* __restrict__ w2,
                                                 char* __restrict__ ws) {
  extern __shared__ __attribute__((aligned(16))) char lds[];
  const int b = blockIdx.x, tid = threadIdx.x, l = tid & 63;
  const int w = __builtin_amdgcn_readfirstlane(tid >> 6);
  const int xg = b & 7;
  TS(2, 0);
  const int hl = (b >> 3) & 3, head = 4 * xg + hl, qb = b >> 5;
  const int r32 = l & 31, h = l >> 5;
  const int qp = w & 3, kh = w >> 2;
  const char* kvh = ws + WS_KV + (size_t)head * KV_HEAD;
  const int tq = qb * 8 + 2 * qp;
  u32x4 qla, qlb; unsigned qha, qhb;
  {
    const char* pq = ws + WS_QLO + (size_t)head * 65536 + tq * 1024 + l * 16;
    const char* ph = ws + WS_QHI + (size_t)head * 8192 + (tq * 32 + r32) * 4;
    asm volatile("global_load_dwordx4 %0, %1, off" : "=v"(qla) : "v"(pq) : "memory");
    asm volatile("global_load_dwordx4 %0, %1, off offset:1024" : "=v"(qlb) : "v"(pq) : "memory");
    asm volatile("global_load_dword %0, %1, off" : "=v"(qha) : "v"(ph) : "memory");
    asm volatile("global_load_dword %0, %1, off offset:128" : "=v"(qhb) : "v"(ph) : "memory");
  }
  const unsigned lds0 = (unsigned)(uintptr_t)lds;
#pragma unroll
  for (int G = 0; G < 2; ++G)
#pragma unroll
    for (int m = 0; m < 5; ++m) {
      const int i = w + 16 * m;
      int off;
      if (m < 2) off = ((i >> 3) * 16 + 8 * G + (i & 7)) * 1024;
      else if (i < 40) off = KV_KHI + (((i - 32) >> 1) * 16 + 8 * G) * 256 + ((i - 32) & 1) * 1024;
      else off = KV_VT + (((i - 40) / 10) * 16 + 8 * G) * VT_TILE + ((i - 40) % 10) * 1024;
      glds16(kvh + off + l * 16, (unsigned)__builtin_amdgcn_readfirstlane(lds0 + off));
    }
  if (tid < 128) *(u32x4*)(lds + L_ZERO + tid * 16) = u32x4{0, 0, 0, 0};
  asm volatile("s_waitcnt vmcnt(5) lgkmcnt(0)\n\ts_barrier" : "+v"(qla), "+v"(qlb), "+v"(qha), "+v"(qhb) :: "memory");
  TS(2, 1);
  {
    const int ru = (b >> 3) * 8 + (w - 8);
    if (w >= 8 && ru < 173) {
      const Unit u = retile_unit(ru, xg, l, wo, w1, w2, ws);
      u32x4 rin[8];
      unit_load(u, rin);
      if (ru < 153) {
        const __amdgpu_buffer_rsrc_t rs = ws_rsrc(ws);
        const unsigned p = (unsigned)(u.p - ws);
#pragma unroll
        for (int i = 0; i < 8; ++i) st_dev(rs, p + u.s1 * (i & 3) + u.s2 * (i >> 2), unit_col(rin, i));
      } else if (ru < 172 || l < 8) {
#pragma unroll
        for (int i = 0; i < 8; ++i) *(u32x4*)(u.p + u.s1 * (i & 3) + u.s2 * (i >> 2)) = unit_col(rin, i);
      }
    }
  }
  SB();
  const bool vz = r32 > 18;
  unsigned ak = lds0 + l * 16, ah = lds0 + KV_KHI + r32 * 8, av = lds0 + KV_VT + h * 304 + r32 * 16;
  const unsigned sv = vz ? 0u : (unsigned)VT_TILE, sh = h ? 0u : 256u;
  u32x2 qa = u32x2{h == 0 ? qha : 0u, 0u}, qb2 = u32x2{h == 0 ? qhb : 0u, 0u};
  const f32x16 zero16 = {0, 0, 0, 0, 0, 0, 0, 0, 0, 0, 0, 0, 0, 0, 0, 0};
  auto rowmax = [&](const f32x16& s0) {
    float m = s0[0];
#pragma unroll
    for (int i = 1; i < 16; ++i) m = fmaxf(m, s0[i]);
    auto rr = __builtin_amdgcn_permlane32_swap(__float_as_uint(m), __float_as_uint(m), false, false);
    return fmaxf(__uint_as_float(rr[0]), __uint_as_float(rr[1]));
  };
  {
    const u32x4 kl0 = ldsr4(ak);
    const u32x2 kh0 = ldsr2(ah);
    f32x16 s0 = mfma32k8(kh0, qa, mfma32(kl0, qla, zero16));
    f32x16 s1 = mfma32k8(kh0, qb2, mfma32(kl0, qlb, zero16));
    const float ma = rowmax(s0), mb = rowmax(s1);
    qa[1] = h == 0 ? (pk2(-ma, 0.f) & 0xffffu) : 0u;
    qb2[1] = h == 0 ? (pk2(-mb, 0.f) & 0xffffu) : 0u;
  }
  ak += kh * 16 * 1024; ah += kh * 16 * 256; av += kh * 16 * VT_TILE;
  if (vz) av = lds0 + L_ZERO;
  if (h) ah = lds0 + L_ZERO;
  asm volatile("" : "+v"(ak), "+v"(ah), "+v"(av));
  u32x4 kl = ldsr4(ak), v0 = ldsr4(av), v1 = ldsr4(av + 608);
  u32x2 kt = ldsr2(ah);
  f32x16 sa = mfma32k8(kt, qa, mfma32(kl, qla, zero16)), sb;
  f32x16 oa = zero16, ob = zero16;
  auto phase = [&](const f32x16& sc, f32x16& sn, f32x16& o, const u32x4& qn, const u32x2& qt, auto first) {
    constexpr bool F = decltype(first)::value;
    float e[16];
    u32x4 p0, p1;
#pragma unroll
    for (int i = 0; i < 5; ++i) e[i] = fast_exp2(sc[i]);
    SB();
    sn = mfma32(kl, qn, zero16);
    if (F) kl = ldsr4(ak + 1024);
    SB();
#pragma unroll
    for (int i = 5; i < 8; ++i) e[i] = fast_exp2(sc[i]);
#pragma unroll
    for (int j2 = 0; j2 < 4; ++j2) p0[j2] = pk2(e[2 * j2], e[2 * j2 + 1]);
    SB();
    o = mfma32(v0, p0, o);
    if (!F) v0 = ldsr4(av + VT_TILE);
    SB();
#pragma unroll
    for (int i = 8; i < 13; ++i) e[i] = fast_exp2(sc[i]);
    SB();
    sn = mfma32k8(kt, qt, sn);
    if (F) kt = ldsr2(ah + 256);
    SB();
#pragma unroll
    for (int i = 13; i < 16; ++i) e[i] = fast_exp2(sc[i]);
#pragma unroll
    for (int j2 = 0; j2 < 4; ++j2) p1[j2] = pk2(e[8 + 2 * j2], e[8 + 2 * j2 + 1]);
    SB();
    o = mfma32(v1, p1, o);
    if (!F) v1 = ldsr4(av + VT_TILE + 608);
    SB();
  };
#pragma unroll
  for (int G = 0; G < 2; ++G) {
    if (G == 1) asm volatile("s_waitcnt vmcnt(0)\n\ts_barrier" ::: "memory");
    TS(2, 2 + G);
    const int n = G == 0 ? 7 : 9;
#pragma unroll 1
    for (int i = 0; i < n; ++i) {
      phase(sa, sb, oa, qlb, qb2, std::true_type{});
      phase(sb, sa, ob, qla, qa, std::false_type{});
      ak += 1024; ah += sh; av += sv;
    }
  }
  TS(2, 6);
  asm volatile("s_barrier" ::: "memory");
  {
    float* ex = (float*)lds + (w * 22) * 64 + l;
#pragma unroll
    for (int i = 0; i < 11; ++i) { if (kh != 0) ex[i * 64] = oa[i]; if (kh != 1) ex[(11 + i) * 64] = ob[i]; }
  }
  __syncthreads();
  if (kh >= 2) return;
  f32x16 o = kh ? ob : oa;
#pragma unroll
  for (int q = 1; q < 4; ++q) {
    const float* ex = (const float*)lds + ((((kh + q) & 3) * 4 + qp) * 22 + kh * 11) * 64 + l;
#pragma unroll
    for (int i = 0; i < 11; ++i) o[i] += ex[i * 64];
  }
  {
    bool bad = false;
#pragma unroll
    for (int i = 0; i < 11; ++i) bad |= !(fabsf(o[i]) < 3.0e38f);
    if (__any(bad)) {
      const u32x4 ql = kh ? qlb : qla;
      const u32x2 qt = u32x2{kh ? qb2[0] : qa[0], 0u};
      float m = -3.0e38f;
      o = zero16;
      for (int t = 0; t < 64; ++t) {
        const u32x4 gkl = *(const u32x4*)(kvh + t * 1024 + l * 16);
        const u32x2 gkh = *(const u32x2*)(kvh + KV_KHI + t * 256 + r32 * 8);
        const char* pv = kvh + KV_VT + t * VT_TILE + h * 304 + min(r32, 18) * 16;
        const u32x4 gv0 = *(const u32x4*)pv, gv1 = *(const u32x4*)(pv + 608);
        const f32x16 s = mfma32k8(gkh, qt, mfma32(gkl, ql, zero16));
        const float mn = fmaxf(m, rowmax(s)), al = fast_exp2(m - mn);
        m = mn;
#pragma unroll
        for (int i = 0; i < 16; ++i) o[i] *= al;
        u32x4 p0, p1;
#pragma unroll
        for (int j2 = 0; j2 < 4; ++j2) {
          p0[j2] = pk2(fast_exp2(s[2 * j2] - m), fast_exp2(s[2 * j2 + 1] - m));
          p1[j2] = pk2(fast_exp2(s[8 + 2 * j2] - m), fast_exp2(s[8 + 2 * j2 + 1] - m));
        }
        o = mfma32(gv0, p0, o);
        o = mfma32(gv1, p1, o);
      }
    }
  }
  auto lrow = __builtin_amdgcn_permlane32_swap(__float_as_uint(o[10]), __float_as_uint(o[10]), false, false);
  const float inv = 1.f / __uint_as_float(lrow[0]);
  const unsigned a0 = pk2(o[0] * inv, o[1] * inv), a1 = pk2(o[2] * inv, o[3] * inv);
  const unsigned b0 = pk2(o[4] * inv, o[5] * inv), b1 = pk2(o[6] * inv, o[7] * inv);
  const unsigned tail = pk2(o[8] * inv, o[9] * inv);
  auto r0 = __builtin_amdgcn_permlane32_swap(a0, b0, false, false);
  auto r1 = __builtin_amdgcn_permlane32_swap(a1, b1, false, false);
  const int tok = (tq + kh) * 32 + r32, mb = tok >> 4, row = tok & 15;
  char* ot = ws + WS_OT + (size_t)mb * 18432 + row * 16;
  const int bi = xg * 9 + 2 * hl + h, bt = xg * 9 + 8;
  *(u32x4*)(ot + (bi >> 2) * 1024 + (bi & 3) * 256) = u32x4{r0[0], r1[0], r0[1], r1[1]};
  if (h == 0) *(unsigned*)(ot + (bt >> 2) * 1024 + (bt & 3) * 256 + hl * 4) = tail;
  TS(2, 7);
}

constexpr int L_H = 0, L_X = 73728, L_STASH = 147456, L_GS = 156672, L_MLP = 161280;

constexpr int MLP_NS = 5;
template <int NB, int P0, int P1, int PEND, int POFF, bool BMAP>
DI void mlp_gemm(const char* __restrict__ wt, const char* A, int l, u32x4 (&wf)[MLP_NS][NB], f32x4 (&acc)[4][NB]) {
  constexpr int NS = MLP_NS;
  auto kmap = [](int p) { return BMAP ? (p < 8 ? p : (p < 10 ? p + 8 : p - 2)) : p; };
  u32x4 hf[2][4];
#pragma unroll
  for (int mb = 0; mb < 4; ++mb) hf[P0 & 1][mb] = *(const u32x4*)(A + mb * 18432 + kmap(P0) * 1024 + l * 16);
#pragma unroll
  for (int p = P0; p < P1; ++p) {
#pragma unroll
    for (int nb = 0; nb < NB; ++nb) {
      if (nb < 4 && p + 1 < P1) hf[(p + 1) & 1][nb] = *(const u32x4*)(A + nb * 18432 + kmap(p + 1) * 1024 + l * 16);
#pragma unroll
      for (int mb = 0; mb < 4; ++mb) acc[mb][nb] = mfma16(wf[(p + POFF) % NS][nb], hf[p & 1][mb], acc[mb][nb]);
      if (p + NS < PEND) wf[(p + POFF) % NS][nb] = *(const u32x4*)(wt + ((p + NS) * NB + nb) * 1024);
      SB();
    }
  }
}

template <int NB>
DI void mlp_body(char* __restrict__ ws, char* lds, const char* __restrict__ wt, u32x4 (&wf)[MLP_NS][NB], int xg, int r, int s, int l) {
  const int g = l >> 4, n = l & 15;
  f32x4 acc[4][NB];
#pragma unroll
  for (int mb = 0; mb < 4; ++mb)
#pragma unroll
    for (int nb = 0; nb < NB; ++nb) acc[mb][nb] = f32x4{0, 0, 0, 0};
  if (NB == 5) __builtin_amdgcn_s_setprio(2);
  mlp_gemm<NB, 0, 18, 36, 0, false>(wt, lds + L_H, l, wf, acc);
  TS(4, 5);
  if (NB == 4) asm volatile("s_barrier" ::: "memory");
  {
    char* X = lds + L_X;
    const int grp = NB == 5 ? s : 4 + s;
#pragma unroll
    for (int mb = 0; mb < 4; ++mb) {
      u32x2 xp[NB];
#pragma unroll
      for (int nb = 0; nb < NB; ++nb) {
        xp[nb] = u32x2{pk2(silu_f(acc[mb][nb][0]), silu_f(acc[mb][nb][1])), pk2(silu_f(acc[mb][nb][2]), silu_f(acc[mb][nb][3]))};
        acc[mb][nb] = f32x4{0, 0, 0, 0};
      }
#pragma unroll
      for (int p = 0; p < 2; ++p)
        *(u32x4*)(X + (mb * 18 + 2 * grp + p) * 1024 + l * 16) = u32x4{xp[2 * p][0], xp[2 * p][1], xp[2 * p + 1][0], xp[2 * p + 1][1]};
      if (NB == 5) *(u32x2*)(X + (mb * 18 + 16 + (s >> 1)) * 1024 + l * 16 + (s & 1) * 8) = xp[NB - 1];
    }
  }
  TS(4, 6);
  asm volatile("s_waitcnt lgkmcnt(0)\n\ts_barrier" ::: "memory");
  if (NB == 5) {
    mlp_gemm<NB, 0, 10, 18, 18, true>(wt + 18 * NB * 1024, lds + L_X, l, wf, acc);
    asm volatile("s_barrier" ::: "memory");
    mlp_gemm<NB, 10, 18, 18, 18, true>(wt + 18 * NB * 1024, lds + L_X, l, wf, acc);
  } else {
    mlp_gemm<NB, 0, 18, 18, 18, true>(wt + 18 * NB * 1024, lds + L_X, l, wf, acc);
  }
  TS(4, 7);
  const __amdgpu_buffer_rsrc_t rs = ws_rsrc(ws);
  const unsigned pbase = (unsigned)WS_PART + (unsigned)((xg * 32 + r) * 8) * 9216u;
  const int grp = NB == 5 ? s : 4 + s;
#pragma unroll
  for (int mb = 0; mb < 4; ++mb) {
#pragma unroll
    for (int q = 0; q < 4; q += 2) {
      const u32x2 h0 = u32x2{pk2(acc[mb][0][q], acc[mb][1][q]), pk2(acc[mb][2][q], acc[mb][3][q])};
      const u32x2 h1 = u32x2{pk2(acc[mb][0][q + 1], acc[mb][1][q + 1]), pk2(acc[mb][2][q + 1], acc[mb][3][q + 1])};
      const int ch = 8 * grp + 4 * (g & 1) + q + (g >> 1);
      st_dev(rs, pbase + (2 * mb + (n >> 3)) * 9216 + ch * 128 + (n & 7) * 16, join32(h0, h1));
    }
  }
  if (NB == 5) {
#pragma unroll
    for (int mb = 0; mb < 4; mb += 2) {
      const u32x2 h0 = u32x2{pk2(acc[mb][NB - 1][0], acc[mb][NB - 1][1]), pk2(acc[mb][NB - 1][2], acc[mb][NB - 1][3])};
      const u32x2 h1 = u32x2{pk2(acc[mb + 1][NB - 1][0], acc[mb + 1][NB - 1][1]), pk2(acc[mb + 1][NB - 1][2], acc[mb + 1][NB - 1][3])};
      const int ch = 64 + 2 * s + (g >> 1);
      st_dev(rs, pbase + (2 * (mb + (g & 1)) + (n >> 3)) * 9216 + ch * 128 + (n & 7) * 16, join16(h0, h1));
    }
    __builtin_amdgcn_s_setprio(0);
  }
}

template <int NB>
DI void mlp_all(const u16* __restrict__ x, char* __restrict__ ws, u16* __restrict__ out, char* lds, const int w) {
  const int b = blockIdx.x, tid = threadIdx.x, l = tid & 63;
  const int xg = (b & 6) + ((b >> 3) & 1), r = 2 * (b >> 4) + (b & 1), s = w & 3;
  const int g = l >> 4, n = l & 15;
  gu32* cnt1 = (gu32*)(ws + WS_FLAG) + r * 64;
  gu32* cnt2 = (gu32*)(ws + WS_FLAG) + (32 + r) * 64;
  gu32* tmo = (gu32*)(ws + WS_FLAG) + 64 * 64;
  const __amdgpu_buffer_rsrc_t rs = ws_rsrc(ws);
  TS(4, 0);
  const char* wt = ws + WS_W12 + (size_t)xg * 1296 * 1024 + (size_t)(NB == 5 ? s * 180 : 720 + s * 144) * 1024 + l * 16;
  u32x4 wf[MLP_NS][NB];
  const int rh = (xg >> 1) & 1, cq = 2 * (xg >> 2) + (xg & 1);
  const float* mod = (const float*)(ws + WS_MOD);
  {
    const int gi = min(tid, 287);
    const f32x4 gv = *(const f32x4*)(mod + (gi < 144 ? 4 * 576 + gi * 4 : 3 * 576 + gi * 4 - 576));
    SB();
    char* O = lds + L_X;
    const char* src = ws + WS_OT + (size_t)(4 * r + 2 * rh) * 18432 + l * 16;
#pragma unroll
    for (int i = 0; i < 5; ++i) {
      const int pc = min(w + 8 * i, 35);
      glds16(src + pc * 1024, (unsigned)__builtin_amdgcn_readfirstlane((unsigned)(uintptr_t)O + pc * 1024));
    }
    u32x4 wo[18], wo8[3];
    const char* wot = ws + WS_WO + (size_t)(cq * 9 * 18) * 1024 + l * 16;
#pragma unroll
    for (int ks = 0; ks < 18; ++ks) wo[ks] = *(const u32x4*)(wot + (w * 18 + ks) * 1024);
#pragma unroll
    for (int i = 0; i < 3; ++i) wo8[i] = *(const u32x4*)(wot + (8 * 18 + min(w + 8 * i, 17)) * 1024);
    u32x2 xr[2];
#pragma unroll
    for (int mb = 0; mb < 2; ++mb) xr[mb] = *(const u32x2*)(x + (size_t)(r * 64 + rh * 32 + mb * 16 + n) * D_ + cq * 144 + w * 16 + 4 * g);
    const f32x4 gav = *(const f32x4*)(mod + 2 * 576 + cq * 144 + w * 16 + 4 * g);
    u32x2 xr8[2]; f32x4 gav8;
    if (NB == 5) {
#pragma unroll
      for (int mb = 0; mb < 2; ++mb) xr8[mb] = *(const u32x2*)(x + (size_t)(r * 64 + rh * 32 + mb * 16 + n) * D_ + cq * 144 + 128 + 4 * g);
      gav8 = *(const f32x4*)(mod + 2 * 576 + cq * 144 + 128 + 4 * g);
    }
    asm volatile("s_barrier" ::: "memory");
#pragma unroll
    for (int p = 0; p < MLP_NS; ++p)
#pragma unroll
      for (int nb = 0; nb < NB; ++nb) wf[p][nb] = *(const u32x4*)(wt + (p * NB + nb) * 1024);
    SB();
    asm volatile("" : "+v"(wo[0]));
    if (tid < 288) *(f32x4*)(lds + L_GS + tid * 16) = gv;
    __syncthreads();
    TS(4, 1);
    f32x4 acc[2] = {{0, 0, 0, 0}, {0, 0, 0, 0}}, a8[2] = {{0, 0, 0, 0}, {0, 0, 0, 0}};
#pragma unroll
    for (int i = 0; i < 3; ++i) {
      const int ks = w + 8 * i;
      if (i < 2 || w < 2) {
#pragma unroll
        for (int mb = 0; mb < 2; ++mb) a8[mb] = mfma16(wo8[i], *(const u32x4*)(O + (mb * 18 + ks) * 1024 + l * 16), a8[mb]);
      }
    }
#pragma unroll
    for (int mb = 0; mb < 2; ++mb) *(f32x4*)(O + 36864 + (w * 2 + mb) * 1024 + l * 16) = a8[mb];
#pragma unroll
    for (int ks = 0; ks < 18; ++ks)
#pragma unroll
      for (int mb = 0; mb < 2; ++mb) acc[mb] = mfma16(wo[ks], *(const u32x4*)(O + (mb * 18 + ks) * 1024 + l * 16), acc[mb]);
    auto x1pk = [&](const f32x4& a, const u32x2& xv, const f32x4& gg) {
      return u32x2{pk2(lo2f(xv[0]) + gg[0] * a[0], hi2f(xv[0]) + gg[1] * a[1]), pk2(lo2f(xv[1]) + gg[2] * a[2], hi2f(xv[1]) + gg[3] * a[3])};
    };
    const unsigned x1b = (unsigned)WS_X1 + r * 73728u + (2 * rh + (g & 1)) * 18432u + n * 16;
    st_dev(rs, x1b + (cq * 18 + 2 * w + (g >> 1)) * 256, join16(x1pk(acc[0], xr[0], gav), x1pk(acc[1], xr[1], gav)));
    __syncthreads();
    if (NB == 5 && w == 0) {
#pragma unroll
      for (int mb = 0; mb < 2; ++mb) {
        a8[mb] = f32x4{0, 0, 0, 0};
#pragma unroll
        for (int i = 0; i < 8; ++i) a8[mb] += *(const f32x4*)(O + 36864 + (i * 2 + mb) * 1024 + l * 16);
      }
      st_dev(rs, x1b + (cq * 18 + 16 + (g >> 1)) * 256, join16(x1pk(a8[0], xr8[0], gav8), x1pk(a8[1], xr8[1], gav8)));
    }
  }
  TS(4, 2);
  publish(cnt1);
  await(cnt1, 8, tmo);
  TS(4, 3);
  {
    u32x4 xv[9];
    const unsigned src = (unsigned)WS_X1 + r * 73728u + (w >> 1) * 18432u + ((w & 1) * 8 + (l & 7)) * 16 + (l >> 3) * 256;
#pragma unroll
    for (int i = 0; i < 9; ++i) xv[i] = ld_dev(rs, src + i * 2048);
    SB();
    float sm = 0.f, sq = 0.f;
#pragma unroll
    for (int i = 0; i < 9; ++i)
#pragma unroll
      for (int j = 0; j < 4; ++j) { sm = dot2(xv[i][j], 0x3f803f80u, sm); sq = dot2(xv[i][j], xv[i][j], sq); }
    if (w == xg) {
#pragma unroll
      for (int i = 0; i < 9; ++i) *(u32x4*)(lds + L_STASH + ((l >> 3) + 8 * i) * 128 + (l & 7) * 16) = xv[i];
    }
    const float mu = red_sub(sm) * (1.f / D_);
    const float var = fmaxf(red_sub(sq) * (1.f / D_) - mu * mu, 0.f);
    const float rstd = 1.f / sqrtf(var + LN_EPS);
    ln_apply(xv, f32x2{rstd, -mu * rstd}, (const float*)(lds + L_GS), lds + L_H, w, l);
  }
  __syncthreads();
  TS(4, 4);
  mlp_body<NB>(ws, lds, wt, wf, xg, r, s, l);
  TS(4, 8);
  publish(cnt2);
  f32x4 gbv[2][2];
#pragma unroll
  for (int c = 0; c < 2; ++c) {
    gbv[c][0] = *(const f32x4*)(mod + 5 * 576 + ((min(tid, 287) + 288 * c) >> 3) * 8);
    gbv[c][1] = *(const f32x4*)(mod + 5 * 576 + ((min(tid, 287) + 288 * c) >> 3) * 8 + 4);
  }
  await(cnt2, 8, tmo);
  TS(4, 9);
  if (tid < 288) {
    u32x4 pp[2][8];
    const unsigned pb = (unsigned)WS_PART + (unsigned)(r * 8 + xg) * 9216u + tid * 16;
#pragma unroll
    for (int c = 0; c < 2; ++c)
#pragma unroll
      for (int sl = 0; sl < 8; ++sl) pp[c][sl] = ld_dev(rs, pb + sl * (32 * 8 * 9216u) + c * 4608);
    SB();
#pragma unroll
    for (int c = 0; c < 2; ++c) {
      const int idx = tid + 288 * c;
      const u32x4 xv = *(const u32x4*)(lds + L_STASH + idx * 16);
      u32x4 o;
#pragma unroll
      for (int j = 0; j < 4; ++j) {
        float a = 0.f, cc = 0.f;
#pragma unroll
        for (int sl = 0; sl < 8; ++sl) { a += lo2f(pp[c][sl][j]); cc += hi2f(pp[c][sl][j]); }
        o[j] = pk2(lo2f(xv[j]) + gbv[c][j >> 1][(2 * j) & 3] * a, hi2f(xv[j]) + gbv[c][j >> 1][(2 * j + 1) & 3] * cc);
      }
      *(u32x4*)(out + (size_t)(r * 64 + xg * 8 + (idx & 7)) * D_ + (idx >> 3) * 8) = o;
    }
  }
  TS(4, 10);
  __builtin_amdgcn_s_waitcnt(0);
}

__global__ __launch_bounds__(512, 2) void k_mlp(const u16* __restrict__ x, char* __restrict__ ws, u16* __restrict__ out) {
  extern __shared__ __attribute__((aligned(16))) char lds[];
  const int w = __builtin_amdgcn_readfirstlane(threadIdx.x >> 6);
  if (w < 4) mlp_all<5>(x, ws, out, lds, w);
  else mlp_all<4>(x, ws, out, lds, w);
}

extern "C" void kernel_launch(void* const* d_in, const int* in_sizes, int n_in, void* d_out, int out_size, void* d_ws, size_t ws_size,
                              hipStream_t stream) {
  const u16* x = (const u16*)d_in[0];
  const u16* wqkv = (const u16*)d_in[1];
  const u16* wo = (const u16*)d_in[2];
  const u16* w1 = (const u16*)d_in[4];
  const u16* w2 = (const u16*)d_in[5];
  const u16* c = (const u16*)d_in[6];
  const u16* wmod = (const u16*)d_in[7];
  char* ws = (char*)d_ws;
  (void)hipFuncSetAttribute((const void*)k_attn, hipFuncAttributeMaxDynamicSharedMemorySize, 163840);
  (void)hipFuncSetAttribute((const void*)k_mlp, hipFuncAttributeMaxDynamicSharedMemorySize, L_MLP);
  k_prep<<<256, 448, 0, stream>>>(x, wqkv, c, wmod, ws);
  k_qkv<<<256, 512, 0, stream>>>(x, ws);
  k_attn<<<256, 1024, 163840, stream>>>(wo, w1, w2, ws);
  k_mlp<<<256, 512, L_MLP, stream>>>(x, ws, (u16*)d_out);
}
```

```cpp
#include <hip/hip_runtime.h>
#include <hip/hip_bf16.h>
#include <stdint.h>
#include <type_traits>

typedef __attribute__((ext_vector_type(8))) __bf16 bf16x8;
typedef __attribute__((ext_vector_type(2))) __bf16 bf16x2;
typedef __attribute__((ext_vector_type(4))) short s16x4;
typedef __attribute__((ext_vector_type(2))) float f32x2;
typedef __attribute__((ext_vector_type(4))) float f32x4;
typedef __attribute__((ext_vector_type(16))) float f32x16;
typedef __attribute__((ext_vector_type(2))) unsigned u32x2;
typedef __attribute__((ext_vector_type(4))) unsigned u32x4;
typedef unsigned short u16;
typedef __attribute__((address_space(1))) unsigned gu32;

#define DI __device__ __forceinline__
#define SB() __builtin_amdgcn_sched_barrier(0)

constexpr int S_ = 2048, D_ = 576, F_ = 4608;
constexpr float LN_EPS = 1e-5f;
constexpr float QSCALE = 0.23570226039551584f * 1.4426950408889634f;

constexpr size_t WS_FLAG = 0;
constexpr size_t WS_MOD  = 28672;
constexpr size_t WS_STAT = 45056;
constexpr size_t WS_WQKV = 61440;
constexpr size_t WS_QLO  = WS_WQKV + 2064384;
constexpr size_t WS_QHI  = WS_QLO + 2097152;
constexpr int    KV_HEAD = 159744, KV_KHI = 65536, KV_VT = 81920, VT_TILE = 1216;
constexpr size_t WS_KV   = WS_QHI + 262144;
constexpr size_t WS_OT   = WS_KV + 32 * (size_t)KV_HEAD;
constexpr size_t WS_WO   = WS_OT + 2359296;
constexpr size_t WS_W12  = WS_WO + 663552;
constexpr size_t WS_X1   = WS_W12 + 2 * 5308416;
constexpr size_t WS_PART = WS_X1 + 2359296;
constexpr size_t WS_END  = WS_PART + 18874368;

#define TS(K, I)

DI float bf2f(u16 v) { return __uint_as_float((unsigned)v << 16); }
DI float lo2f(unsigned v) { return __uint_as_float(v << 16); }
DI float hi2f(unsigned v) { return __uint_as_float(v & 0xffff0000u); }
DI unsigned pk2(float a, float b) { bf16x2 v = {(__bf16)a, (__bf16)b}; return __builtin_bit_cast(unsigned, v); }
DI float rbf(float a) { return (float)(__bf16)a; }
DI f32x4 mfma16(u32x4 a, u32x4 b, f32x4 c) {
  return __builtin_amdgcn_mfma_f32_16x16x32_bf16(__builtin_bit_cast(bf16x8, a), __builtin_bit_cast(bf16x8, b), c, 0, 0, 0);
}
DI f32x16 mfma32(u32x4 a, u32x4 b, f32x16 c) {
  return __builtin_amdgcn_mfma_f32_32x32x16_bf16(__builtin_bit_cast(bf16x8, a), __builtin_bit_cast(bf16x8, b), c, 0, 0, 0);
}
DI f32x16 mfma32k8(u32x2 a, u32x2 b, f32x16 c) {
  return __builtin_amdgcn_mfma_f32_32x32x8bf16_1k(__builtin_bit_cast(s16x4, a), __builtin_bit_cast(s16x4, b), c, 0, 0, 0);
}
DI float fast_rcp(float x) { return __builtin_amdgcn_rcpf(x); }
DI float fast_exp2(float x) { return __builtin_amdgcn_exp2f(x); }
DI float silu_f(float x) { return x * fast_rcp(1.f + fast_exp2(-1.4426950408889634f * x)); }
DI float xor_add(float v, int m) { return v + __shfl_xor(v, m); }
DI float dot2(unsigned a, unsigned b, float c) {
  return __builtin_amdgcn_fdot2_f32_bf16(__builtin_bit_cast(bf16x2, a), __builtin_bit_cast(bf16x2, b), c, false);
}
DI float red_sub(float v) {
  v += __builtin_bit_cast(float, __builtin_amdgcn_update_dpp(0, __builtin_bit_cast(int, v), 0x128, 0xf, 0xf, false));
  auto a = __builtin_amdgcn_permlane16_swap(__float_as_uint(v), __float_as_uint(v), false, false);
  v = __uint_as_float(a[0]) + __uint_as_float(a[1]);
  auto b = __builtin_amdgcn_permlane32_swap(__float_as_uint(v), __float_as_uint(v), false, false);
  return __uint_as_float(b[0]) + __uint_as_float(b[1]);
}
DI u32x4 join16(u32x2 x, u32x2 y) {
  auto a = __builtin_amdgcn_permlane16_swap(x[0], y[0], false, false);
  auto b = __builtin_amdgcn_permlane16_swap(x[1], y[1], false, false);
  return u32x4{a[0], b[0], a[1], b[1]};
}
DI u32x4 join32(u32x2 x, u32x2 y) {
  auto a = __builtin_amdgcn_permlane32_swap(x[0], y[0], false, false);
  auto b = __builtin_amdgcn_permlane32_swap(x[1], y[1], false, false);
  return u32x4{a[0], b[0], a[1], b[1]};
}
DI void glds16(const void* g, unsigned lds_base) {
  unsigned sv;
  asm volatile("s_mov_b32 %0, m0\n\ts_mov_b32 m0, %2\n\ts_nop 0\n\tglobal_load_lds_dwordx4 %1, off\n\ts_mov_b32 m0, %0"
               : "=&s"(sv) : "v"(g), "s"(lds_base) : "memory");
}
DI __amdgpu_buffer_rsrc_t ws_rsrc(char* ws) { return __builtin_amdgcn_make_buffer_rsrc(ws, 0, (int)WS_END, 0x00020000); }
DI void st_dev(__amdgpu_buffer_rsrc_t rs, unsigned off, u32x4 v) { __builtin_amdgcn_raw_buffer_store_b128(v, rs, off, 0, 16); }
DI u32x4 ld_dev(__amdgpu_buffer_rsrc_t rs, unsigned off) { return __builtin_amdgcn_raw_buffer_load_b128(rs, off, 0, 16); }
DI void await(gu32* cnt, unsigned want, gu32* tmo) {
  if (threadIdx.x == 0) {
    for (unsigned spins = 0; __hip_atomic_load(cnt, __ATOMIC_RELAXED, __HIP_MEMORY_SCOPE_AGENT) < want;) {
      __builtin_amdgcn_s_sleep(1);
      if (++spins > (1u << 20)) { __hip_atomic_store(tmo, 1u, __ATOMIC_RELAXED, __HIP_MEMORY_SCOPE_AGENT); break; }
    }
  }
  __builtin_amdgcn_fence(__ATOMIC_ACQUIRE, "wavefront");
  __syncthreads();
}

struct Unit { const u16* src; char* p; int ld, a, b, c, s1, s2; };
DI void unit_load(const Unit& u, u32x4 (&in)[8]) {
#pragma unroll
  for (int j = 0; j < 8; ++j) in[j] = *(const u32x4*)(u.src + (size_t)(u.a * (j >> 2) + u.b * ((j >> 1) & 1) + u.c * (j & 1)) * u.ld);
}
DI u32x4 unit_col(const u32x4 (&in)[8], int i) {
  u32x4 o;
#pragma unroll
  for (int m = 0; m < 4; ++m) o[m] = __builtin_amdgcn_perm(in[2 * m + 1][i >> 1], in[2 * m][i >> 1], (i & 1) ? 0x07060302u : 0x05040100u);
  return o;
}

__global__ __launch_bounds__(256) void k_prep(const u16* __restrict__ x, const u16* __restrict__ wqkv, const u16* __restrict__ c,
                                              const u16* __restrict__ wmod, char* __restrict__ ws) {
  __shared__ float red[128 * 16];
  const int tid = threadIdx.x, l = tid & 63;
  const int w = __builtin_amdgcn_readfirstlane(tid >> 6);
  if (blockIdx.x < 216) {
    const int b = blockIdx.x;
    TS(0, 0);
    const int cg = (b & 7) * 27 + (b >> 3);
    const int half = tid & 1, row0 = tid >> 1;
    u32x4 wv[5]; u16 cv[5];
#pragma unroll
    for (int i = 0; i < 5; ++i) {
      const int k = min(row0 + 128 * i, 575);
      wv[i] = *(const u32x4*)(wmod + (size_t)k * 3456 + cg * 16 + half * 8);
      cv[i] = c[k];
    }
    float acc[8];
#pragma unroll
    for (int j = 0; j < 8; ++j) acc[j] = 0.f;
#pragma unroll
    for (int i = 0; i < 5; ++i) {
      float s = rbf(silu_f(bf2f(cv[i])));
      if (row0 + 128 * i >= 576) s = 0.f;
#pragma unroll
      for (int j = 0; j < 4; ++j) { acc[2 * j] += s * lo2f(wv[i][j]); acc[2 * j + 1] += s * hi2f(wv[i][j]); }
    }
    *(f32x4*)&red[row0 * 16 + half * 8] = f32x4{acc[0], acc[1], acc[2], acc[3]};
    *(f32x4*)&red[row0 * 16 + half * 8 + 4] = f32x4{acc[4], acc[5], acc[6], acc[7]};
    __syncthreads();
    if (w == 0) {
      const int col = l & 15, part = l >> 4;
      float s = 0.f;
#pragma unroll
      for (int i = 0; i < 32; ++i) s += red[(part * 32 + i) * 16 + col];
      s = xor_add(s, 16); s = xor_add(s, 32);
      if (l < 16) {
        const int n = cg * 16 + col, sec = n / 576;
        float v = rbf(s);
        if (sec == 1 || sec == 4) v = rbf(1.f + v);
        ((float*)(ws + WS_MOD))[n] = v;
      }
    }
    TS(0, 1);
    return;
  }
  const int b = blockIdx.x - 216;
  if (w == 0) {
    const int row = 8 * b + (l & 7), sub = l >> 3;
    u32x4 xv[9];
#pragma unroll
    for (int i = 0; i < 9; ++i) xv[i] = *(const u32x4*)(x + (size_t)row * D_ + (sub + 8 * i) * 8);
    float s = 0.f;
#pragma unroll
    for (int i = 0; i < 9; ++i)
#pragma unroll
      for (int j = 0; j < 4; ++j) s += lo2f(xv[i][j]) + hi2f(xv[i][j]);
    const float mu = red_sub(s) * (1.f / D_);
    float q = 0.f;
#pragma unroll
    for (int i = 0; i < 9; ++i)
#pragma unroll
      for (int j = 0; j < 4; ++j) { const float a = lo2f(xv[i][j]) - mu, bb = hi2f(xv[i][j]) - mu; q += a * a + bb * bb; }
    q = red_sub(q);
    const float rstd = 1.f / sqrtf(q * (1.f / D_) + LN_EPS);
    if (sub == 0) ((f32x2*)(ws + WS_STAT))[row] = f32x2{rstd, -mu * rstd};
  } else if (w == 3) {
    if (l < 28) ((unsigned*)(ws + WS_FLAG))[b * 28 + l] = 0u;
    {
      unsigned lo, hi;
      asm volatile("s_getpc_b64 s[20:21]\n\ts_add_u32 s20, s20, _Z5k_qkvPKtPc@rel32@lo+4\n\ts_addc_u32 s21, s21, _Z5k_qkvPKtPc@rel32@hi+12\n\t"
                   "s_mov_b32 %0, s20\n\ts_mov_b32 %1, s21" : "=s"(lo), "=s"(hi) :: "s20", "s21", "scc");
      const char* code = (const char*)(((unsigned long long)hi << 32) | lo);
      const char* p = code + min((b >> 3) * 64 + l, 967) * 64;
      unsigned j0;
      asm volatile("global_load_dword %0, %1, off" : "=&v"(j0) : "v"(p) : "memory");
    }
  } else {
    const int xg = b & 7, rank = b >> 3;
    char* dst = ws + WS_WQKV + (size_t)xg * 252 * 1024;
    if (w == 1) {
      if (l < 54) {
        const int i = rank * 54 + l, kg = i / 24, rem = i % 24, part = rem >> 3, hl = (rem >> 1) & 3, half = rem & 1;
        Unit u;
        u.src = wqkv + (size_t)kg * 8 * 1728 + part * 576 + (4 * xg + hl) * 18 + half * 8;
        u.ld = 1728; u.a = 4; u.b = 2; u.c = 1;
        char* p = dst + ((kg >> 2) * 14 + (part < 2 ? 2 * hl + part : 8 + hl)) * 1024 + ((kg & 3) * 16 + half * 8) * 16;
        u32x4 in[8];
        unit_load(u, in);
#pragma unroll
        for (int i2 = 0; i2 < 8; ++i2) *(u32x4*)(p + i2 * 16) = unit_col(in, i2);
      }
    } else {
      if (l < 9) {
        const int i = rank * 9 + l, kg = i >> 2, hl = i & 3;
        const u16* src = wqkv + (size_t)kg * 8 * 1728 + (4 * xg + hl) * 18 + 16;
        unsigned in[3][8];
#pragma unroll
        for (int p = 0; p < 3; ++p)
#pragma unroll
          for (int j = 0; j < 8; ++j) in[p][j] = *(const unsigned*)(src + (size_t)j * 1728 + p * 576);
        char* pq = dst + ((kg >> 2) * 14 + 12) * 1024 + (kg & 3) * 256;
#pragma unroll
        for (int p = 0; p < 3; ++p)
#pragma unroll
          for (int e = 0; e < 2; ++e) {
            u32x4 o;
#pragma unroll
            for (int m = 0; m < 4; ++m) o[m] = __builtin_amdgcn_perm(in[p][2 * m + 1], in[p][2 * m], e ? 0x07060302u : 0x05040100u);
            if (p < 2) *(u32x4*)(pq + (4 * hl + 2 * p + e) * 16) = o;
            else { *(u32x4*)(pq + 1024 + (2 * hl + e) * 16) = o; *(u32x4*)(pq + 1024 + (8 + 2 * hl + e) * 16) = u32x4{0, 0, 0, 0}; }
          }
      }
    }
  }
}

DI void ln_apply(const u32x4 (&xv)[9], f32x2 st, const float* gs, char* A, int w, int l) {
  const int sub = l >> 3;
  f32x4 gq[2][4];
  auto fetch = [&](int i, f32x4 (&q)[4]) {
    const float* p = gs + (sub + 8 * i) * 8;
    q[0] = *(const f32x4*)p; q[1] = *(const f32x4*)(p + 4); q[2] = *(const f32x4*)(p + 576); q[3] = *(const f32x4*)(p + 580);
  };
  fetch(0, gq[0]);
#pragma unroll
  for (int i = 0; i < 9; ++i) {
    if (i + 1 < 9) fetch(i + 1, gq[(i + 1) & 1]);
    SB();
    const f32x4 (&q)[4] = gq[i & 1];
    u32x4 o;
#pragma unroll
    for (int j = 0; j < 4; ++j) {
      const f32x2 gg = {q[j >> 1][(2 * j) & 3], q[j >> 1][(2 * j + 1) & 3]}, ss = {q[2 + (j >> 1)][(2 * j) & 3], q[2 + (j >> 1)][(2 * j + 1) & 3]};
      const f32x2 xx = {lo2f(xv[i][j]), hi2f(xv[i][j])};
      const f32x2 a = (xx * f32x2{st[0], st[0]} + f32x2{st[1], st[1]}) * gg + ss;
      o[j] = pk2(a[0], a[1]);
    }
    *(u32x4*)(A + (w >> 1) * 18432 + (sub + 8 * i) * 256 + ((w & 1) * 8 + (l & 7)) * 16) = o;
    SB();
  }
}

template <bool TFORM>
DI void qkv_gemm(const char* __restrict__ wt, const char* A, int s, int l, f32x4 (&a0)[4], f32x4 (&a1)[4], f32x4& at,
                 u32x4 (&wf)[6][3]) {
  constexpr int NS = 6;
  u32x4 hf[2][4];
#pragma unroll
  for (int i = 0; i < 4; ++i) hf[0][i] = *(const u32x4*)(A + ((i + s) & 3) * 18432 + l * 16);
#pragma unroll
  for (int ks = 0; ks < 18; ++ks) {
    const int kk = ks % NS;
#pragma unroll
    for (int i = 0; i < 4; ++i) {
      if (ks + 1 < 18) hf[(ks + 1) & 1][i] = *(const u32x4*)(A + ((i + s) & 3) * 18432 + (ks + 1) * 1024 + l * 16);
      if (TFORM) {
        a0[i] = mfma16(wf[kk][0], hf[ks & 1][i], a0[i]);
        a1[i] = mfma16(wf[kk][1], hf[ks & 1][i], a1[i]);
        if (i == 0) at = mfma16(wf[kk][2], hf[ks & 1][0], at);
      } else {
        a0[i] = mfma16(hf[ks & 1][i], wf[kk][0], a0[i]);
        if (i == 0) at = mfma16(hf[ks & 1][0], wf[kk][2], at);
      }
      SB();
    }
    if (ks + NS < 18) {
      wf[kk][0] = *(const u32x4*)(wt + (ks + NS) * 14336);
      if (TFORM) wf[kk][1] = *(const u32x4*)(wt + (ks + NS) * 14336 + 1024);
      wf[kk][2] = *(const u32x4*)(wt + (ks + NS) * 14336 + (TFORM ? 12 - 2 * s : 13 - 8 - s) * 1024);
    }
    SB();
  }
}

template <bool TFORM>
DI void qkv_all(const u16* __restrict__ x, char* __restrict__ ws, char* A, float* gs, const int w) {
  const int b = blockIdx.x, tid = threadIdx.x, l = tid & 63;
  const int xg = (b & 6) + ((b >> 3) & 1), r = 2 * (b >> 4) + (b & 1), s = w & 3;
  TS(1, 0);
  u32x4 xv[9];
  const int row = r * 64 + 8 * w + (l & 7);
#pragma unroll
  for (int i = 0; i < 9; ++i) xv[i] = *(const u32x4*)(x + (size_t)row * D_ + ((l >> 3) + 8 * i) * 8);
  const f32x2 st = ((const f32x2*)(ws + WS_STAT))[row];
  const int gi = min(tid, 287);
  const f32x4 gv = *(const f32x4*)((const float*)(ws + WS_MOD) + (gi < 144 ? 576 + gi * 4 : gi * 4 - 576));
  asm volatile("s_barrier" ::: "memory");
  const char* wt = ws + WS_WQKV + (size_t)xg * 252 * 1024 + (TFORM ? 2 * s : 8 + s) * 1024 + l * 16;
  u32x4 wf[6][3];
#pragma unroll
  for (int ks = 0; ks < 6; ++ks) {
    wf[ks][0] = *(const u32x4*)(wt + ks * 14336);
    if (TFORM) wf[ks][1] = *(const u32x4*)(wt + ks * 14336 + 1024);
    wf[ks][2] = *(const u32x4*)(wt + ks * 14336 + (TFORM ? 12 - 2 * s : 13 - 8 - s) * 1024);
  }
  SB();
  if (tid < 288) *(f32x4*)(gs + tid * 4) = gv;
  __syncthreads();
  TS(1, 7);
  ln_apply(xv, st, gs, A, w, l);
  TS(1, 1);
  __syncthreads();
  TS(1, 2);
  f32x4 a0[4], a1[4], at = {0, 0, 0, 0};
#pragma unroll
  for (int i = 0; i < 4; ++i) { a0[i] = at; a1[i] = at; }
  const int g = l >> 4, n = l & 15;
  const int head = 4 * xg + s;
  char* kvh = ws + WS_KV + (size_t)head * KV_HEAD;
  qkv_gemm<TFORM>(wt, A, s, l, a0, a1, at, wf);
  TS(1, 3);
  if (TFORM) {
    char* qlo = ws + WS_QLO + (size_t)head * 65536;
#pragma unroll
    for (int i = 0; i < 4; i += 2) {
      const int mb = (i + (g & 1) + s) & 3;
      const int off = (2 * r + (mb >> 1)) * 1024 + (g >> 1) * 512 + (16 * (mb & 1) + n) * 16;
      u32x2 q0, q1, k0, k1;
      q0 = u32x2{pk2(a0[i][0] * QSCALE, a0[i][1] * QSCALE), pk2(a0[i][2] * QSCALE, a0[i][3] * QSCALE)};
      q1 = u32x2{pk2(a0[i + 1][0] * QSCALE, a0[i + 1][1] * QSCALE), pk2(a0[i + 1][2] * QSCALE, a0[i + 1][3] * QSCALE)};
      k0 = u32x2{pk2(a1[i][0], a1[i][1]), pk2(a1[i][2], a1[i][3])};
      k1 = u32x2{pk2(a1[i + 1][0], a1[i + 1][1]), pk2(a1[i + 1][2], a1[i + 1][3])};
      *(u32x4*)(qlo + off) = join16(q0, q1);
      *(u32x4*)(kvh + off) = join16(k0, k1);
    }
    const int tok = r * 64 + s * 16 + n, hg = 4 * xg + g;
    ((unsigned*)(ws + WS_QHI))[hg * 2048 + tok] = pk2(at[0] * QSCALE, at[1] * QSCALE);
    *(u32x2*)(ws + WS_KV + (size_t)hg * KV_HEAD + KV_KHI + tok * 8) = u32x2{pk2(at[2], at[3]), 0x00003f80u};
  } else {
#pragma unroll
    for (int i = 0; i < 4; i += 2) {
      const int mb = (i + (g >> 1) + s) & 3;
      const u32x2 v0 = u32x2{pk2(a0[i][0], a0[i][1]), pk2(a0[i][2], a0[i][3])};
      const u32x2 v1 = u32x2{pk2(a0[i + 1][0], a0[i + 1][1]), pk2(a0[i + 1][2], a0[i + 1][3])};
      *(u32x4*)(kvh + KV_VT + (2 * r + (mb >> 1)) * VT_TILE + (mb & 1) * 608 + (g & 1) * 304 + n * 16) = join32(v0, v1);
    }
    if (n < 12) {
      const int hg = 4 * xg + (n < 8 ? n >> 1 : n - 8), vr = n < 8 ? 16 + (n & 1) : 18;
      const u32x2 v = n < 8 ? u32x2{pk2(at[0], at[1]), pk2(at[2], at[3])} : u32x2{0x3f803f80u, 0x3f803f80u};
      *(u32x2*)(ws + WS_KV + (size_t)hg * KV_HEAD + KV_VT + (2 * r + (s >> 1)) * VT_TILE + (s & 1) * 608 + (g & 1) * 304 + vr * 16 + (g >> 1) * 8) = v;
    }
  }
  TS(1, 4);
  __builtin_amdgcn_s_waitcnt(0);
  if (TFORM) asm volatile("; end of role T" ::: "memory"); else asm volatile("; end of role N" ::: "memory");
}

__global__ __launch_bounds__(512, 2) void k_qkv(const u16* __restrict__ x, char* __restrict__ ws) {
  __shared__ __attribute__((aligned(16))) char A[73728];
  __shared__ __attribute__((aligned(16))) float gs[1152];
  const int w = __builtin_amdgcn_readfirstlane(threadIdx.x >> 6);
  if (w < 4) qkv_all<true>(x, ws, A, gs, w);
  else qkv_all<false>(x, ws, A, gs, w);
}

DI size_t w12_frag(int grp, int j, int step) {
  return grp < 4 ? grp * 180 + step * 5 + j : (grp < 8 ? 720 + (grp - 4) * 144 + step * 4 + j : j * 180 + step * 5 + 4);
}
struct RUnit { __amdgpu_buffer_rsrc_t rs; unsigned off, da, db, dc, p, s1, s2; };
DI RUnit retile_unit(int u0, int xg, int l, const u16* __restrict__ wo, const u16* __restrict__ w1, const u16* __restrict__ w2) {
  RUnit u;
  const int cl = l & 7, kgl = l >> 3;
  const unsigned w12 = (unsigned)WS_W12 + (unsigned)xg * 1296 * 1024;
  if (u0 < 81) {
    const int grp = u0 / 9, kg = (u0 % 9) * 8 + kgl, ks = kg >> 2, kq = kg & 3;
    u.rs = __builtin_amdgcn_make_buffer_rsrc((void*)w1, 0, D_ * F_ * 2, 0x00020000);
    u.off = (kg * 8 * F_ + xg * 576 + grp * 64 + cl * 8) * 2; u.da = 4 * F_ * 2; u.db = 2 * F_ * 2; u.dc = F_ * 2;
    u.p = w12 + (unsigned)w12_frag(grp, 0, ks) * 1024 + kq * 256 + cl * 16;
    u.s1 = grp < 8 ? 1024 : 180 * 1024; u.s2 = 128;
  } else if (u0 < 162) {
    const int u2 = u0 - 81, grp = u2 / 9, kg = (u2 % 9) * 8 + kgl, ks = kg >> 2, kq = kg & 3;
    const int row = (ks >> 1) * 64 + 32 * (kq & 1) + 4 * (kq >> 1) + 2 * (ks & 1);
    const int step = 18 + (ks < 8 ? ks : (ks < 16 ? ks + 2 : ks - 8));
    u.rs = __builtin_amdgcn_make_buffer_rsrc((void*)w2, 0, D_ * F_ * 2, 0x00020000);
    u.off = ((xg * 576 + row) * D_ + grp * 64 + cl * 8) * 2; u.da = D_ * 2; u.db = 16 * D_ * 2; u.dc = 8 * D_ * 2;
    if (grp < 8) { u.p = w12 + (unsigned)w12_frag(grp, 0, step) * 1024 + kq * 256 + cl * 16; u.s1 = 1024; u.s2 = 128; }
    else { u.p = w12 + (unsigned)w12_frag(8, cl >> 1, step) * 1024 + kq * 256 + (cl & 1) * 128; u.s1 = 16; u.s2 = 64; }
  } else {
    const int i3 = min(xg * 648 + (u0 - 162) * 64 + l, 5183), kg = i3 / 72, cg = i3 % 72, ks = kg >> 2, kq = kg & 3;
    const int xx = kg / 9, uu = kg % 9;
    const int row = uu < 8 ? (4 * xx + (uu >> 1)) * 18 + 8 * (uu & 1) : 4 * xx * 18 + 16;
    u.rs = __builtin_amdgcn_make_buffer_rsrc((void*)wo, 0, D_ * D_ * 2, 0x00020000);
    u.off = (row * D_ + cg * 8) * 2; u.da = (uu < 8 ? 4 : 36) * D_ * 2; u.db = (uu < 8 ? 2 : 18) * D_ * 2; u.dc = D_ * 2;
    u.p = (unsigned)WS_WO + ((cg >> 1) * 18 + ks) * 1024 + (kq * 16 + (cg & 1) * 8) * 16;
    u.s1 = 16; u.s2 = 64;
  }
  return u;
}

constexpr int L_ZERO = 160768;
constexpr int L_JUNK = 162816;
constexpr int MLP_CODE_LINES = 628;
typedef __attribute__((address_space(3))) u32x4 lds_u32x4;
typedef __attribute__((address_space(3))) u32x2 lds_u32x2;
DI u32x4 ldsr4(unsigned a) { return *(const lds_u32x4*)(uintptr_t)a; }
DI u32x2 ldsr2(unsigned a) { return *(const lds_u32x2*)(uintptr_t)a; }

__global__ __launch_bounds__(1024, 4) void k_attn(const u16* __restrict__ wo, const u16* __restrict__ w1, const u16* __restrict__ w2,
                                                 char* __restrict__ ws) {
  extern __shared__ __attribute__((aligned(16))) char lds[];
  const int b = blockIdx.x, tid = threadIdx.x, l = tid & 63;
  const int w = __builtin_amdgcn_readfirstlane(tid >> 6);
  const int xg = b & 7;
  TS(2, 0);
  const int hl = (b >> 3) & 3, head = 4 * xg + hl, qb = b >> 5;
  const int r32 = l & 31, h = l >> 5;
  const int qp = w & 3, kh = w >> 2;
  const char* kvh = ws + WS_KV + (size_t)head * KV_HEAD;
  const int tq = qb * 8 + 2 * qp;
  u32x4 qla, qlb; unsigned qha, qhb;
  {
    const char* pq = ws + WS_QLO + (size_t)head * 65536 + tq * 1024 + l * 16;
    const char* ph = ws + WS_QHI + (size_t)head * 8192 + (tq * 32 + r32) * 4;
    asm volatile("global_load_dwordx4 %0, %1, off" : "=v"(qla) : "v"(pq) : "memory");
    asm volatile("global_load_dwordx4 %0, %1, off offset:1024" : "=v"(qlb) : "v"(pq) : "memory");
    asm volatile("global_load_dword %0, %1, off" : "=v"(qha) : "v"(ph) : "memory");
    asm volatile("global_load_dword %0, %1, off offset:128" : "=v"(qhb) : "v"(ph) : "memory");
  }
  const unsigned lds0 = (unsigned)(uintptr_t)lds;
#pragma unroll
  for (int G = 0; G < 2; ++G)
#pragma unroll
    for (int m = 0; m < 5; ++m) {
      const int i = w + 16 * m;
      int off;
      if (m < 2) off = ((i >> 3) * 16 + 8 * G + (i & 7)) * 1024;
      else if (i < 40) off = KV_KHI + (((i - 32) >> 1) * 16 + 8 * G) * 256 + ((i - 32) & 1) * 1024;
      else off = KV_VT + (((i - 40) / 10) * 16 + 8 * G) * VT_TILE + ((i - 40) % 10) * 1024;
      glds16(kvh + off + l * 16, (unsigned)__builtin_amdgcn_readfirstlane(lds0 + off));
    }
  if (tid < 128) *(u32x4*)(lds + L_ZERO + tid * 16) = u32x4{0, 0, 0, 0};
  asm volatile("s_waitcnt vmcnt(5) lgkmcnt(0)\n\ts_barrier" : "+v"(qla), "+v"(qlb), "+v"(qha), "+v"(qhb) :: "memory");
  TS(2, 1);
  const int rw = w & 7, ru = rw < 5 ? (b >> 3) * 5 + rw : 153 + (b >> 3);
  const bool rdo = rw < 5 ? ru < 153 : (rw == 5 && ru < 173);
  if (w >= 14) {
    unsigned lo, hi, sv;
    asm volatile("s_getpc_b64 s[20:21]\n\ts_add_u32 s20, s20, _Z5k_mlpPKtPcPt@rel32@lo+4\n\ts_addc_u32 s21, s21, _Z5k_mlpPKtPcPt@rel32@hi+12\n\t"
                 "s_mov_b32 %0, s20\n\ts_mov_b32 %1, s21" : "=s"(lo), "=s"(hi) :: "s20", "s21", "scc");
    const char* p = (const char*)(((unsigned long long)hi << 32) | lo) + min(((b >> 3) * 2 + (w - 14)) * 10 + min(l, 9), MLP_CODE_LINES - 1) * 64;
    asm volatile("s_mov_b32 %0, m0\n\ts_mov_b32 m0, %2\n\ts_nop 0\n\tglobal_load_lds_dword %1, off\n\ts_mov_b32 m0, %0"
                 : "=&s"(sv) : "v"(p), "s"(lds0 + L_JUNK) : "memory");
  } else if (w >= 8) {
    const RUnit u = retile_unit(rdo ? ru : 0, xg, l & 56, wo, w1, w2);
    const unsigned off = u.off + u.da * ((l >> 2) & 1) + u.db * ((l >> 1) & 1) + u.dc * (l & 1);
    unsigned sv;
    asm volatile("s_mov_b32 %0, m0\n\ts_mov_b32 m0, %3\n\ts_nop 0\n\tbuffer_load_dword %1, %2, 0 offen lds\n\ts_mov_b32 m0, %0"
                 : "=&s"(sv) : "v"(off), "s"(u.rs), "s"(lds0 + L_JUNK) : "memory");
  }
  SB();
  const bool vz = r32 > 18;
  unsigned ak = lds0 + l * 16, ah = lds0 + KV_KHI + r32 * 8, av = lds0 + KV_VT + h * 304 + r32 * 16;
  const unsigned sv = vz ? 0u : (unsigned)VT_TILE, sh = h ? 0u : 256u;
  u32x2 qa = u32x2{h == 0 ? qha : 0u, 0u}, qb2 = u32x2{h == 0 ? qhb : 0u, 0u};
  const f32x16 zero16 = {0, 0, 0, 0, 0, 0, 0, 0, 0, 0, 0, 0, 0, 0, 0, 0};
  auto rowmax = [&](const f32x16& s0) {
    float m = s0[0];
#pragma unroll
    for (int i = 1; i < 16; ++i) m = fmaxf(m, s0[i]);
    auto rr = __builtin_amdgcn_permlane32_swap(__float_as_uint(m), __float_as_uint(m), false, false);
    return fmaxf(__uint_as_float(rr[0]), __uint_as_float(rr[1]));
  };
  {
    const u32x4 kl0 = ldsr4(ak);
    const u32x2 kh0 = ldsr2(ah);
    f32x16 s0 = mfma32k8(kh0, qa, mfma32(kl0, qla, zero16));
    f32x16 s1 = mfma32k8(kh0, qb2, mfma32(kl0, qlb, zero16));
    const float ma = rowmax(s0), mb = rowmax(s1);
    qa[1] = h == 0 ? (pk2(-ma, 0.f) & 0xffffu) : 0u;
    qb2[1] = h == 0 ? (pk2(-mb, 0.f) & 0xffffu) : 0u;
  }
  ak += kh * 16 * 1024; ah += kh * 16 * 256; av += kh * 16 * VT_TILE;
  if (vz) av = lds0 + L_ZERO;
  if (h) ah = lds0 + L_ZERO;
  asm volatile("" : "+v"(ak), "+v"(ah), "+v"(av));
  u32x4 kl = ldsr4(ak), v0 = ldsr4(av), v1 = ldsr4(av + 608);
  u32x2 kt = ldsr2(ah);
  f32x16 sa = mfma32k8(kt, qa, mfma32(kl, qla, zero16)), sb;
  f32x16 oa = zero16, ob = zero16;
  auto phase = [&](const f32x16& sc, f32x16& sn, f32x16& o, const u32x4& qn, const u32x2& qt, auto first) {
    constexpr bool F = decltype(first)::value;
    float e[16];
    u32x4 p0, p1;
#pragma unroll
    for (int i = 0; i < 5; ++i) e[i] = fast_exp2(sc[i]);
    SB();
    sn = mfma32(kl, qn, zero16);
    if (F) kl = ldsr4(ak + 1024);
    SB();
#pragma unroll
    for (int i = 5; i < 8; ++i) e[i] = fast_exp2(sc[i]);
#pragma unroll
    for (int j2 = 0; j2 < 4; ++j2) p0[j2] = pk2(e[2 * j2], e[2 * j2 + 1]);
    SB();
    o = mfma32(v0, p0, o);
    if (!F) v0 = ldsr4(av + VT_TILE);
    SB();
#pragma unroll
    for (int i = 8; i < 13; ++i) e[i] = fast_exp2(sc[i]);
    SB();
    sn = mfma32k8(kt, qt, sn);
    if (F) kt = ldsr2(ah + 256);
    SB();
#pragma unroll
    for (int i = 13; i < 16; ++i) e[i] = fast_exp2(sc[i]);
#pragma unroll
    for (int j2 = 0; j2 < 4; ++j2) p1[j2] = pk2(e[8 + 2 * j2], e[8 + 2 * j2 + 1]);
    SB();
    o = mfma32(v1, p1, o);
    if (!F) v1 = ldsr4(av + VT_TILE + 608);
    SB();
  };
#pragma unroll
  for (int G = 0; G < 2; ++G) {
    if (G == 1) {
      if (w < 8) asm volatile("s_waitcnt vmcnt(0)\n\ts_barrier" ::: "memory");
      else asm volatile("s_waitcnt vmcnt(1)\n\ts_barrier" ::: "memory");
      if (w < 8 && rdo) {
        const RUnit u = retile_unit(ru, xg, l, wo, w1, w2);
        u32x4 rin[8];
#pragma unroll
        for (int j = 0; j < 8; ++j)
          rin[j] = __builtin_amdgcn_raw_buffer_load_b128(u.rs, u.off + u.da * (j >> 2) + u.db * ((j >> 1) & 1) + u.dc * (j & 1), 0, 0);
        const __amdgpu_buffer_rsrc_t rs = ws_rsrc(ws);
        if (rw < 5) {
#pragma unroll
          for (int i = 0; i < 8; ++i) st_dev(rs, u.p + u.s1 * (i & 3) + u.s2 * (i >> 2), unit_col(rin, i));
        } else if (ru < 172 || l < 8) {
#pragma unroll
          for (int i = 0; i < 8; ++i) __builtin_amdgcn_raw_buffer_store_b128(unit_col(rin, i), rs, u.p + u.s1 * (i & 3) + u.s2 * (i >> 2), 0, 0);
        }
      }
    }
    TS(2, 2 + G);
#pragma unroll 1
    for (int q = 0; q < 4; ++q) {
      if (q == 0) __builtin_amdgcn_s_setprio(3);
      else if (q == 1) __builtin_amdgcn_s_setprio(2);
      else if (q == 2) __builtin_amdgcn_s_setprio(1);
      else __builtin_amdgcn_s_setprio(0);
      const int n = G == 0 ? (q < 3 ? 2 : 1) : (q == 0 ? 3 : 2);
#pragma unroll 1
      for (int i = 0; i < n; ++i) {
        phase(sa, sb, oa, qlb, qb2, std::true_type{});
        phase(sb, sa, ob, qla, qa, std::false_type{});
        ak += 1024; ah += sh; av += sv;
      }
    }
  }
  TS(2, 6);
  asm volatile("s_waitcnt vmcnt(0)\n\ts_barrier" ::: "memory");
  {
    float* ex = (float*)lds + (w * 22) * 64 + l;
#pragma unroll
    for (int i = 0; i < 11; ++i) { if (kh != 0) ex[i * 64] = oa[i]; if (kh != 1) ex[(11 + i) * 64] = ob[i]; }
  }
  __syncthreads();
  if (kh >= 2) return;
  f32x16 o = kh ? ob : oa;
#pragma unroll
  for (int q = 1; q < 4; ++q) {
    const float* ex = (const float*)lds + ((((kh + q) & 3) * 4 + qp) * 22 + kh * 11) * 64 + l;
#pragma unroll
    for (int i = 0; i < 11; ++i) o[i] += ex[i * 64];
  }
  {
    bool bad = false;
#pragma unroll
    for (int i = 0; i < 11; ++i) bad |= !(fabsf(o[i]) < 3.0e38f);
    if (__any(bad)) {
      const u32x4 ql = kh ? qlb : qla;
      const u32x2 qt = u32x2{kh ? qb2[0] : qa[0], 0u};
      float m = -3.0e38f;
      o = zero16;
      for (int t = 0; t < 64; ++t) {
        const u32x4 gkl = *(const u32x4*)(kvh + t * 1024 + l * 16);
        const u32x2 gkh = *(const u32x2*)(kvh + KV_KHI + t * 256 + r32 * 8);
        const char* pv = kvh + KV_VT + t * VT_TILE + h * 304 + min(r32, 18) * 16;
        const u32x4 gv0 = *(const u32x4*)pv, gv1 = *(const u32x4*)(pv + 608);
        const f32x16 s = mfma32k8(gkh, qt, mfma32(gkl, ql, zero16));
        const float mn = fmaxf(m, rowmax(s)), al = fast_exp2(m - mn);
        m = mn;
#pragma unroll
        for (int i = 0; i < 16; ++i) o[i] *= al;
        u32x4 p0, p1;
#pragma unroll
        for (int j2 = 0; j2 < 4; ++j2) {
          p0[j2] = pk2(fast_exp2(s[2 * j2] - m), fast_exp2(s[2 * j2 + 1] - m));
          p1[j2] = pk2(fast_exp2(s[8 + 2 * j2] - m), fast_exp2(s[8 + 2 * j2 + 1] - m));
        }
        o = mfma32(gv0, p0, o);
        o = mfma32(gv1, p1, o);
      }
    }
  }
  auto lrow = __builtin_amdgcn_permlane32_swap(__float_as_uint(o[10]), __float_as_uint(o[10]), false, false);
  const float inv = 1.f / __uint_as_float(lrow[0]);
  const unsigned a0 = pk2(o[0] * inv, o[1] * inv), a1 = pk2(o[2] * inv, o[3] * inv);
  const unsigned b0 = pk2(o[4] * inv, o[5] * inv), b1 = pk2(o[6] * inv, o[7] * inv);
  const unsigned tail = pk2(o[8] * inv, o[9] * inv);
  auto r0 = __builtin_amdgcn_permlane32_swap(a0, b0, false, false);
  auto r1 = __builtin_amdgcn_permlane32_swap(a1, b1, false, false);
  const int tok = (tq + kh) * 32 + r32, mb = tok >> 4, row = tok & 15;
  char* ot = ws + WS_OT + (size_t)mb * 18432 + row * 16;
  const int bi = xg * 9 + 2 * hl + h, bt = xg * 9 + 8;
  *(u32x4*)(ot + (bi >> 2) * 1024 + (bi & 3) * 256) = u32x4{r0[0], r1[0], r0[1], r1[1]};
  if (h == 0) *(unsigned*)(ot + (bt >> 2) * 1024 + (bt & 3) * 256 + hl * 4) = tail;
  TS(2, 7);
}

constexpr int L_H = 0, L_X = 73728, L_STASH = 147456, L_GS = 156672, L_SYNC = 161280, L_MLP = 161344;

constexpr int MLP_NS0 = 3;
template <int NB> constexpr int mlp_ns() { return NB == 5 ? 5 : 8; }
template <int NB, int P0, int P1, int PEND, int POFF, bool BMAP>
DI void mlp_gemm(__amdgpu_buffer_rsrc_t rs, unsigned wv, unsigned a, u32x4 (&wf)[mlp_ns<NB>()][NB], f32x4 (&acc)[4][NB]) {
  constexpr int NS = mlp_ns<NB>();
  auto kmap = [](int p) { return BMAP ? (p < 8 ? p : (p < 10 ? p + 8 : p - 2)) : p; };
  unsigned a3 = a + 3 * 18432;
  asm volatile("" : "+v"(a), "+v"(a3));
  auto hload = [&](int mb, int p) { return mb < 3 ? ldsr4(a + mb * 18432 + kmap(p) * 1024) : ldsr4(a3 + kmap(p) * 1024); };
  auto refill = [&](int p, int nb) {
    if (p + NS < PEND)
      wf[(p + POFF) % NS][nb] = __builtin_amdgcn_raw_buffer_load_b128(rs, wv + (nb & 3) * 1024, ((p + POFF + NS) * NB + (nb & 4)) * 1024, 0);
  };
  u32x4 hf[2][4];
#pragma unroll
  for (int mb = 0; mb < 4; ++mb) hf[P0 & 1][mb] = hload(mb, P0);
#pragma unroll
  for (int p = P0; p < P1; ++p) {
#pragma unroll
    for (int nb = 0; nb < NB; ++nb) {
      const u32x4 wc = wf[(p + POFF) % NS][nb];
      SB();
      acc[0][nb] = mfma16(wc, hf[p & 1][0], acc[0][nb]);
      SB();
      if (nb > 0) refill(p, nb - 1); else if (p > P0) refill(p - 1, NB - 1);
      SB();
      acc[1][nb] = mfma16(wc, hf[p & 1][1], acc[1][nb]);
      SB();
      if (nb < 2 && p + 1 < P1) hf[(p + 1) & 1][2 * nb] = hload(2 * nb, p + 1);
      SB();
      acc[2][nb] = mfma16(wc, hf[p & 1][2], acc[2][nb]);
      SB();
      if (nb < 2 && p + 1 < P1) hf[(p + 1) & 1][2 * nb + 1] = hload(2 * nb + 1, p + 1);
      SB();
      acc[3][nb] = mfma16(wc, hf[p & 1][3], acc[3][nb]);
      SB();
    }
  }
  refill(P1 - 1, NB - 1);
  SB();
}

template <int NB>
DI void mlp_body(char* __restrict__ ws, char* lds, unsigned wv, u32x4 (&wf)[mlp_ns<NB>()][NB], int xg, int r, int s, int l) {
  const __amdgpu_buffer_rsrc_t rs = ws_rsrc(ws);
  const unsigned la = (unsigned)(uintptr_t)lds + l * 16;
  const int g = l >> 4, n = l & 15;
  f32x4 acc[4][NB];
#pragma unroll
  for (int mb = 0; mb < 4; ++mb)
#pragma unroll
    for (int nb = 0; nb < NB; ++nb) acc[mb][nb] = f32x4{0, 0, 0, 0};
  if (NB == 5) __builtin_amdgcn_s_setprio(2);
  mlp_gemm<NB, 0, 18, 36, 0, false>(rs, wv, la + L_H, wf, acc);
  TS(4, 5);
  if (NB == 4) asm volatile("s_barrier" ::: "memory");
  {
    char* X = lds + L_X;
    const int grp = NB == 5 ? s : 4 + s;
#pragma unroll
    for (int mb = 0; mb < 4; ++mb) {
      u32x2 xp[NB];
#pragma unroll
      for (int nb = 0; nb < NB; ++nb) {
        xp[nb] = u32x2{pk2(silu_f(acc[mb][nb][0]), silu_f(acc[mb][nb][1])), pk2(silu_f(acc[mb][nb][2]), silu_f(acc[mb][nb][3]))};
        acc[mb][nb] = f32x4{0, 0, 0, 0};
      }
#pragma unroll
      for (int p = 0; p < 2; ++p)
        *(u32x4*)(X + (mb * 18 + 2 * grp + p) * 1024 + l * 16) = u32x4{xp[2 * p][0], xp[2 * p][1], xp[2 * p + 1][0], xp[2 * p + 1][1]};
      if (NB == 5) *(u32x2*)(X + (mb * 18 + 16 + (s >> 1)) * 1024 + l * 16 + (s & 1) * 8) = xp[NB - 1];
    }
  }
  TS(4, 6);
  asm volatile("s_waitcnt lgkmcnt(0)\n\ts_barrier" ::: "memory");
  if (NB == 5) {
    mlp_gemm<NB, 0, 10, 18, 18, true>(rs, wv, la + L_X, wf, acc);
    asm volatile("s_barrier" ::: "memory");
    mlp_gemm<NB, 10, 18, 18, 18, true>(rs, wv, la + L_X, wf, acc);
  } else {
    mlp_gemm<NB, 0, 18, 18, 18, true>(rs, wv, la + L_X, wf, acc);
  }
  TS(4, 7);
  const unsigned pbase = (unsigned)WS_PART + (unsigned)((xg * 32 + r) * 8) * 9216u;
  const int grp = NB == 5 ? s : 4 + s;
#pragma unroll
  for (int mb = 0; mb < 4; ++mb) {
#pragma unroll
    for (int q = 0; q < 4; q += 2) {
      const u32x2 h0 = u32x2{pk2(acc[mb][0][q], acc[mb][1][q]), pk2(acc[mb][2][q], acc[mb][3][q])};
      const u32x2 h1 = u32x2{pk2(acc[mb][0][q + 1], acc[mb][1][q + 1]), pk2(acc[mb][2][q + 1], acc[mb][3][q + 1])};
      const int ch = 8 * grp + 4 * (g & 1) + q + (g >> 1);
      st_dev(rs, pbase + (2 * mb + (n >> 3)) * 9216 + ch * 128 + (n & 7) * 16, join32(h0, h1));
    }
  }
  if (NB == 5) {
#pragma unroll
    for (int mb = 0; mb < 4; mb += 2) {
      const u32x2 h0 = u32x2{pk2(acc[mb][NB - 1][0], acc[mb][NB - 1][1]), pk2(acc[mb][NB - 1][2], acc[mb][NB - 1][3])};
      const u32x2 h1 = u32x2{pk2(acc[mb + 1][NB - 1][0], acc[mb + 1][NB - 1][1]), pk2(acc[mb + 1][NB - 1][2], acc[mb + 1][NB - 1][3])};
      const int ch = 64 + 2 * s + (g >> 1);
      st_dev(rs, pbase + (2 * (mb + (g & 1)) + (n >> 3)) * 9216 + ch * 128 + (n & 7) * 16, join16(h0, h1));
    }
    __builtin_amdgcn_s_setprio(0);
  }
}

template <int NB>
DI void mlp_all(const u16* __restrict__ x, char* __restrict__ ws, u16* __restrict__ out, char* lds, const int w) {
  const int b = blockIdx.x, tid = threadIdx.x, l = tid & 63;
  const int xg = (b & 6) + ((b >> 3) & 1), r = 2 * (b >> 4) + (b & 1), s = w & 3;
  const int g = l >> 4, n = l & 15;
  gu32* cnt1 = (gu32*)(ws + WS_FLAG) + r * 64;
  gu32* tmo = (gu32*)(ws + WS_FLAG) + 96 * 64;
  const __amdgpu_buffer_rsrc_t rs = ws_rsrc(ws);
  TS(4, 0);
  const unsigned wv = (unsigned)WS_W12 + (unsigned)(xg * 1296 + (NB == 5 ? s * 180 : 720 + s * 144)) * 1024u + l * 16;
  u32x4 wf[mlp_ns<NB>()][NB];
  const int rh = (xg >> 1) & 1, cq = 2 * (xg >> 2) + (xg & 1);
  const float* mod = (const float*)(ws + WS_MOD);
  {
    const int gi = min(tid, 287);
    const f32x4 gv = *(const f32x4*)(mod + (gi < 144 ? 4 * 576 + gi * 4 : 3 * 576 + gi * 4 - 576));
    SB();
    char* O = lds + L_X;
    const char* src = ws + WS_OT + (size_t)(4 * r + 2 * rh) * 18432 + l * 16;
#pragma unroll
    for (int i = 0; i < 5; ++i) {
      const int pc = min(w + 8 * i, 35);
      glds16(src + pc * 1024, (unsigned)__builtin_amdgcn_readfirstlane((unsigned)(uintptr_t)O + pc * 1024));
    }
    asm volatile("s_barrier" ::: "memory");
    u32x4 wo[18], wo8[3];
    const char* wot = ws + WS_WO + (size_t)(cq * 9 * 18) * 1024 + l * 16;
#pragma unroll
    for (int i = 0; i < (NB == 5 ? 3 : 2); ++i) wo8[i] = *(const u32x4*)(wot + (8 * 18 + min(w + 8 * i, 17)) * 1024);
#pragma unroll
    for (int ks = 0; ks < 12; ++ks) wo[ks] = *(const u32x4*)(wot + (w * 18 + ks) * 1024);
    SB();
    if (tid < 288) *(f32x4*)(lds + L_GS + tid * 16) = gv;
    if (tid >= 504) *(unsigned*)(lds + L_SYNC + (tid - 504) * 4) = 0u;
    asm volatile("s_waitcnt vmcnt(14) lgkmcnt(0)\n\ts_barrier" ::: "memory");
#pragma unroll
    for (int ks = 12; ks < 18; ++ks) wo[ks] = *(const u32x4*)(wot + (w * 18 + ks) * 1024);
    u32x2 xr[2];
#pragma unroll
    for (int mb = 0; mb < 2; ++mb) xr[mb] = *(const u32x2*)(x + (size_t)(r * 64 + rh * 32 + mb * 16 + n) * D_ + cq * 144 + w * 16 + 4 * g);
    const f32x4 gav = *(const f32x4*)(mod + 2 * 576 + cq * 144 + w * 16 + 4 * g);
    u32x2 xr8[2]; f32x4 gav8;
    if (NB == 5) {
#pragma unroll
      for (int mb = 0; mb < 2; ++mb) xr8[mb] = *(const u32x2*)(x + (size_t)(r * 64 + rh * 32 + mb * 16 + n) * D_ + cq * 144 + 128 + 4 * g);
      gav8 = *(const f32x4*)(mod + 2 * 576 + cq * 144 + 128 + 4 * g);
    }
    SB();
    TS(4, 1);
    f32x4 acc[2] = {{0, 0, 0, 0}, {0, 0, 0, 0}}, a8[2] = {{0, 0, 0, 0}, {0, 0, 0, 0}};
    unsigned oa = (unsigned)(uintptr_t)O + l * 16;
    asm volatile("" : "+v"(oa));
    u32x4 o8[3][2], of[4][2];
#pragma unroll
    for (int i = 0; i < 3; ++i)
#pragma unroll
      for (int mb = 0; mb < 2; ++mb) if (i < 2 || NB == 5) o8[i][mb] = ldsr4(oa + min(w + 8 * i, 17) * 1024 + mb * 18432);
#pragma unroll
    for (int ks = 0; ks < 3; ++ks)
#pragma unroll
      for (int mb = 0; mb < 2; ++mb) of[ks][mb] = ldsr4(oa + (mb * 18 + ks) * 1024);
    SB();
#pragma unroll
    for (int i = 0; i < 3; ++i) {
      if (i < 2 || (NB == 5 && w < 2)) {
#pragma unroll
        for (int mb = 0; mb < 2; ++mb) a8[mb] = mfma16(wo8[i], o8[i][mb], a8[mb]);
      }
    }
#pragma unroll
    for (int mb = 0; mb < 2; ++mb) *(f32x4*)(O + 36864 + (w * 2 + mb) * 1024 + l * 16) = a8[mb];
    SB();
#pragma unroll
    for (int ks = 0; ks < 18; ++ks) {
      if (ks + 3 < 18) {
#pragma unroll
        for (int mb = 0; mb < 2; ++mb) of[(ks + 3) & 3][mb] = ldsr4(oa + (mb * 18 + ks + 3) * 1024);
      }
      SB();
#pragma unroll
      for (int mb = 0; mb < 2; ++mb) acc[mb] = mfma16(wo[ks], of[ks & 3][mb], acc[mb]);
      SB();
    }
    auto x1pk = [&](const f32x4& a, const u32x2& xv, const f32x4& gg) {
      return u32x2{pk2(lo2f(xv[0]) + gg[0] * a[0], hi2f(xv[0]) + gg[1] * a[1]), pk2(lo2f(xv[1]) + gg[2] * a[2], hi2f(xv[1]) + gg[3] * a[3])};
    };
    const unsigned x1b = (unsigned)WS_X1 + r * 73728u + (2 * rh + (g & 1)) * 18432u + n * 16;
    st_dev(rs, x1b + (cq * 18 + 2 * w + (g >> 1)) * 256, join16(x1pk(acc[0], xr[0], gav), x1pk(acc[1], xr[1], gav)));
    asm volatile("s_waitcnt lgkmcnt(0)\n\ts_barrier" ::: "memory");
    if (NB == 5 && w == 0) {
#pragma unroll
      for (int mb = 0; mb < 2; ++mb) {
        a8[mb] = f32x4{0, 0, 0, 0};
#pragma unroll
        for (int i = 0; i < 8; ++i) a8[mb] += *(const f32x4*)(O + 36864 + (i * 2 + mb) * 1024 + l * 16);
      }
      st_dev(rs, x1b + (cq * 18 + 16 + (g >> 1)) * 256, join16(x1pk(a8[0], xr8[0], gav8), x1pk(a8[1], xr8[1], gav8)));
    }
    asm volatile("s_barrier" ::: "memory");
    SB();
#pragma unroll
    for (int q = 0; q < MLP_NS0 * NB; ++q) wf[q / NB][q % NB] = __builtin_amdgcn_raw_buffer_load_b128(rs, wv + (q & 3) * 1024, (q & ~3) * 1024, 0);
    SB();
  }
  TS(4, 2);
  static_assert(MLP_NS0 == 3, "counted wait");
  if (NB == 5) asm volatile("s_waitcnt vmcnt(15)\n\ts_barrier" ::: "memory"); else asm volatile("s_waitcnt vmcnt(12)\n\ts_barrier" ::: "memory");
  if (tid == 0) __hip_atomic_fetch_add(cnt1, 1u, __ATOMIC_RELAXED, __HIP_MEMORY_SCOPE_AGENT);
  await(cnt1, 8, tmo);
  TS(4, 3);
  {
    u32x4 xv[9];
    const unsigned src = (unsigned)WS_X1 + r * 73728u + (w >> 1) * 18432u + ((w & 1) * 8 + (l & 7)) * 16 + (l >> 3) * 256;
#pragma unroll
    for (int i = 0; i < 9; ++i) xv[i] = ld_dev(rs, src + i * 2048);
    SB();
#pragma unroll
    for (int p = MLP_NS0; p < mlp_ns<NB>(); ++p)
#pragma unroll
      for (int nb = 0; nb < NB; ++nb) wf[p][nb] = __builtin_amdgcn_raw_buffer_load_b128(rs, wv + (nb & 3) * 1024, (p * NB + (nb & 4)) * 1024, 0);
    SB();
    float sm = 0.f, sq = 0.f;
#pragma unroll
    for (int i = 0; i < 9; ++i)
#pragma unroll
      for (int j = 0; j < 4; ++j) { sm = dot2(xv[i][j], 0x3f803f80u, sm); sq = dot2(xv[i][j], xv[i][j], sq); }
    if (w == xg) {
#pragma unroll
      for (int i = 0; i < 9; ++i) *(u32x4*)(lds + L_STASH + ((l >> 3) + 8 * i) * 128 + (l & 7) * 16) = xv[i];
    }
    const float mu = red_sub(sm) * (1.f / D_);
    const float var = fmaxf(red_sub(sq) * (1.f / D_) - mu * mu, 0.f);
    const float rstd = 1.f / sqrtf(var + LN_EPS);
    ln_apply(xv, f32x2{rstd, -mu * rstd}, (const float*)(lds + L_GS), lds + L_H, w, l);
  }
  __syncthreads();
  TS(4, 4);
  mlp_body<NB>(ws, lds, wv, wf, xg, r, s, l);
  TS(4, 8);
  const int tl = tid & 255;
  constexpr int NC = NB == 5 ? 2 : 1;
  int chunk[NC];
  f32x4 gbv[NC][2];
#pragma unroll
  for (int c = 0; c < NC; ++c) {
    chunk[c] = NB == 5 ? (c == 0 ? tl : 512 + (tl & 63)) : 256 + tl;
    gbv[c][0] = *(const f32x4*)(mod + 5 * 576 + (chunk[c] >> 3) * 8);
    gbv[c][1] = *(const f32x4*)(mod + 5 * 576 + (chunk[c] >> 3) * 8 + 4);
  }
  {
    gu32* cnt2 = (gu32*)(ws + WS_FLAG) + (32 + r + (NB == 5 ? 0 : 32)) * 64;
    typedef __attribute__((address_space(3))) unsigned lu32;
    lu32* sync = (lu32*)(uintptr_t)((unsigned)(uintptr_t)lds + L_SYNC + (NB == 5 ? 0 : 16));
    asm volatile("s_waitcnt vmcnt(0)" ::: "memory");
    unsigned old = 0;
    if (l == 0) old = __hip_atomic_fetch_add(sync, 1u, __ATOMIC_RELAXED, __HIP_MEMORY_SCOPE_WORKGROUP);
    old = __builtin_amdgcn_readfirstlane(old);
    if (l == 0) {
      if (old == 3) {
        __hip_atomic_fetch_add(cnt2, 1u, __ATOMIC_RELAXED, __HIP_MEMORY_SCOPE_AGENT);
        for (unsigned spins = 0; __hip_atomic_load(cnt2, __ATOMIC_RELAXED, __HIP_MEMORY_SCOPE_AGENT) < 8;) {
          __builtin_amdgcn_s_sleep(1);
          if (++spins > (1u << 20)) { __hip_atomic_store(tmo, 1u, __ATOMIC_RELAXED, __HIP_MEMORY_SCOPE_AGENT); break; }
        }
        __hip_atomic_store(sync + 1, 1u, __ATOMIC_RELAXED, __HIP_MEMORY_SCOPE_WORKGROUP);
      } else {
        for (unsigned spins = 0; __hip_atomic_load(sync + 1, __ATOMIC_RELAXED, __HIP_MEMORY_SCOPE_WORKGROUP) == 0 && spins < (1u << 24); ++spins)
          __builtin_amdgcn_s_sleep(2);
      }
    }
    __builtin_amdgcn_fence(__ATOMIC_ACQUIRE, "wavefront");
    asm volatile("" ::: "memory");
  }
  TS(4, 9);
#pragma unroll
  for (int c = 0; c < NC; ++c) {
    if (c == 1 && w != 0) break;
    u32x4 pp[8];
    const unsigned pb = (unsigned)WS_PART + (unsigned)(r * 8 + xg) * 9216u + chunk[c] * 16;
#pragma unroll
    for (int sl = 0; sl < 8; ++sl) pp[sl] = ld_dev(rs, pb + sl * (32 * 8 * 9216u));
    SB();
    const u32x4 xv = *(const u32x4*)(lds + L_STASH + chunk[c] * 16);
    u32x4 o;
#pragma unroll
    for (int j = 0; j < 4; ++j) {
      float a = 0.f, cc = 0.f;
#pragma unroll
      for (int sl = 0; sl < 8; ++sl) { a += lo2f(pp[sl][j]); cc += hi2f(pp[sl][j]); }
      o[j] = pk2(lo2f(xv[j]) + gbv[c][j >> 1][(2 * j) & 3] * a, hi2f(xv[j]) + gbv[c][j >> 1][(2 * j + 1) & 3] * cc);
    }
    *(u32x4*)(out + (size_t)(r * 64 + xg * 8 + (chunk[c] & 7)) * D_ + (chunk[c] >> 3) * 8) = o;
  }
  TS(4, 10);
  __builtin_amdgcn_s_waitcnt(0);
  if (NB == 5) asm volatile("; end of role A" ::: "memory"); else asm volatile("; end of role B" ::: "memory");
}

__global__ __launch_bounds__(512, 2) void k_mlp(const u16* __restrict__ x, char* __restrict__ ws, u16* __restrict__ out) {
  extern __shared__ __attribute__((aligned(16))) char lds[];
  const int w = __builtin_amdgcn_readfirstlane(threadIdx.x >> 6);
  if (w < 4) mlp_all<5>(x, ws, out, lds, w);
  else mlp_all<4>(x, ws, out, lds, w);
}

extern "C" void kernel_launch(void* const* d_in, const int* in_sizes, int n_in, void* d_out, int out_size, void* d_ws, size_t ws_size,
                              hipStream_t stream) {
  const u16* x = (const u16*)d_in[0];
  const u16* wqkv = (const u16*)d_in[1];
  const u16* wo = (const u16*)d_in[2];
  const u16* w1 = (const u16*)d_in[4];
  const u16* w2 = (const u16*)d_in[5];
  const u16* c = (const u16*)d_in[6];
  const u16* wmod = (const u16*)d_in[7];
  char* ws = (char*)d_ws;
  (void)hipFuncSetAttribute((const void*)k_attn, hipFuncAttributeMaxDynamicSharedMemorySize, 163840);
  (void)hipFuncSetAttribute((const void*)k_mlp, hipFuncAttributeMaxDynamicSharedMemorySize, L_MLP);
  k_prep<<<472, 256, 0, stream>>>(x, wqkv, c, wmod, ws);
  k_qkv<<<256, 512, 0, stream>>>(x, ws);
  k_attn<<<256, 1024, 163840, stream>>>(wo, w1, w2, ws);
  k_mlp<<<256, 512, L_MLP, stream>>>(x, ws, (u16*)d_out);
}
```

```cpp
#include <hip/hip_runtime.h>
#include <hip/hip_bf16.h>
#include <stdint.h>
#include <type_traits>

typedef __attribute__((ext_vector_type(8))) __bf16 bf16x8;
typedef __attribute__((ext_vector_type(2))) __bf16 bf16x2;
typedef __attribute__((ext_vector_type(4))) short s16x4;
typedef __attribute__((ext_vector_type(2))) float f32x2;
typedef __attribute__((ext_vector_type(4))) float f32x4;
typedef __attribute__((ext_vector_type(16))) float f32x16;
typedef __attribute__((ext_vector_type(2))) unsigned u32x2;
typedef __attribute__((ext_vector_type(4))) unsigned u32x4;
typedef unsigned short u16;
typedef __attribute__((address_space(1))) unsigned gu32;

#define DI __device__ __forceinline__
#define SB() __builtin_amdgcn_sched_barrier(0)

constexpr int S_ = 2048, D_ = 576, F_ = 4608;
constexpr float LN_EPS = 1e-5f;
constexpr float QSCALE = 0.23570226039551584f * 1.4426950408889634f;

constexpr size_t WS_FLAG = 0;
constexpr size_t WS_MOD  = 28672;
constexpr size_t WS_STAT = 45056;
constexpr size_t WS_WQKV = 61440;
constexpr size_t WS_QLO  = WS_WQKV + 2064384;
constexpr size_t WS_QHI  = WS_QLO + 2097152;
constexpr int    KV_HEAD = 159744, KV_KHI = 65536, KV_VT = 81920, VT_TILE = 1216;
constexpr size_t WS_KV   = WS_QHI + 262144;
constexpr size_t WS_OT   = WS_KV + 32 * (size_t)KV_HEAD;
constexpr size_t WS_WO   = WS_OT + 2359296;
constexpr size_t WS_W12  = WS_WO + 663552;
constexpr size_t WS_X1   = WS_W12 + 2 * 5308416;
constexpr size_t WS_PART = WS_X1 + 2359296;
constexpr size_t WS_END  = WS_PART + 18874368;

#define TS(K, I)

DI float bf2f(u16 v) { return __uint_as_float((unsigned)v << 16); }
DI float lo2f(unsigned v) { return __uint_as_float(v << 16); }
DI float hi2f(unsigned v) { return __uint_as_float(v & 0xffff0000u); }
DI unsigned pk2(float a, float b) { bf16x2 v = {(__bf16)a, (__bf16)b}; return __builtin_bit_cast(unsigned, v); }
DI float rbf(float a) { return (float)(__bf16)a; }
DI f32x4 mfma16(u32x4 a, u32x4 b, f32x4 c) {
  return __builtin_amdgcn_mfma_f32_16x16x32_bf16(__builtin_bit_cast(bf16x8, a), __builtin_bit_cast(bf16x8, b), c, 0, 0, 0);
}
DI f32x16 mfma32(u32x4 a, u32x4 b, f32x16 c) {
  return __builtin_amdgcn_mfma_f32_32x32x16_bf16(__builtin_bit_cast(bf16x8, a), __builtin_bit_cast(bf16x8, b), c, 0, 0, 0);
}
DI f32x16 mfma32k8(u32x2 a, u32x2 b, f32x16 c) {
  return __builtin_amdgcn_mfma_f32_32x32x8bf16_1k(__builtin_bit_cast(s16x4, a), __builtin_bit_cast(s16x4, b), c, 0, 0, 0);
}
DI float fast_rcp(float x) { return __builtin_amdgcn_rcpf(x); }
DI float fast_exp2(float x) { return __builtin_amdgcn_exp2f(x); }
DI float silu_f(float x) { return x * fast_rcp(1.f + fast_exp2(-1.4426950408889634f * x)); }
DI float xor_add(float v, int m) { return v + __shfl_xor(v, m); }
DI float dot2(unsigned a, unsigned b, float c) {
  return __builtin_amdgcn_fdot2_f32_bf16(__builtin_bit_cast(bf16x2, a), __builtin_bit_cast(bf16x2, b), c, false);
}
DI float red_sub(float v) {
  v += __builtin_bit_cast(float, __builtin_amdgcn_update_dpp(0, __builtin_bit_cast(int, v), 0x128, 0xf, 0xf, false));
  auto a = __builtin_amdgcn_permlane16_swap(__float_as_uint(v), __float_as_uint(v), false, false);
  v = __uint_as_float(a[0]) + __uint_as_float(a[1]);
  auto b = __builtin_amdgcn_permlane32_swap(__float_as_uint(v), __float_as_uint(v), false, false);
  return __uint_as_float(b[0]) + __uint_as_float(b[1]);
}
DI u32x4 join16(u32x2 x, u32x2 y) {
  auto a = __builtin_amdgcn_permlane16_swap(x[0], y[0], false, false);
  auto b = __builtin_amdgcn_permlane16_swap(x[1], y[1], false, false);
  return u32x4{a[0], b[0], a[1], b[1]};
}
DI u32x4 join32(u32x2 x, u32x2 y) {
  auto a = __builtin_amdgcn_permlane32_swap(x[0], y[0], false, false);
  auto b = __builtin_amdgcn_permlane32_swap(x[1], y[1], false, false);
  return u32x4{a[0], b[0], a[1], b[1]};
}
DI void glds16(const void* g, unsigned lds_base) {
  unsigned sv;
  asm volatile("s_mov_b32 %0, m0\n\ts_mov_b32 m0, %2\n\ts_nop 0\n\tglobal_load_lds_dwordx4 %1, off\n\ts_mov_b32 m0, %0"
               : "=&s"(sv) : "v"(g), "s"(lds_base) : "memory");
}
DI __amdgpu_buffer_rsrc_t ws_rsrc(char* ws) { return __builtin_amdgcn_make_buffer_rsrc(ws, 0, (int)WS_END, 0x00020000); }
DI void st_dev(__amdgpu_buffer_rsrc_t rs, unsigned off, u32x4 v) { __builtin_amdgcn_raw_buffer_store_b128(v, rs, off, 0, 16); }
DI u32x4 ld_dev(__amdgpu_buffer_rsrc_t rs, unsigned off) { return __builtin_amdgcn_raw_buffer_load_b128(rs, off, 0, 16); }
DI void await(gu32* cnt, unsigned want, gu32* tmo) {
  if (threadIdx.x == 0) {
    for (unsigned spins = 0; __hip_atomic_load(cnt, __ATOMIC_RELAXED, __HIP_MEMORY_SCOPE_AGENT) < want;) {
      __builtin_amdgcn_s_sleep(1);
      if (++spins > (1u << 20)) { __hip_atomic_store(tmo, 1u, __ATOMIC_RELAXED, __HIP_MEMORY_SCOPE_AGENT); break; }
    }
  }
  __builtin_amdgcn_fence(__ATOMIC_ACQUIRE, "wavefront");
  __syncthreads();
}

struct Unit { const u16* src; char* p; int ld, a, b, c, s1, s2; };
DI void unit_load(const Unit& u, u32x4 (&in)[8]) {
#pragma unroll
  for (int j = 0; j < 8; ++j) in[j] = *(const u32x4*)(u.src + (size_t)(u.a * (j >> 2) + u.b * ((j >> 1) & 1) + u.c * (j & 1)) * u.ld);
}
DI u32x4 unit_col(const u32x4 (&in)[8], int i) {
  u32x4 o;
#pragma unroll
  for (int m = 0; m < 4; ++m) o[m] = __builtin_amdgcn_perm(in[2 * m + 1][i >> 1], in[2 * m][i >> 1], (i & 1) ? 0x07060302u : 0x05040100u);
  return o;
}

__global__ __launch_bounds__(256) void k_prep(const u16* __restrict__ x, const u16* __restrict__ wqkv, const u16* __restrict__ c,
                                              const u16* __restrict__ wmod, char* __restrict__ ws) {
  __shared__ float red[128 * 16];
  const int tid = threadIdx.x, l = tid & 63;
  const int w = __builtin_amdgcn_readfirstlane(tid >> 6);
  if (blockIdx.x < 216) {
    const int b = blockIdx.x;
    TS(0, 0);
    const int cg = (b & 7) * 27 + (b >> 3);
    const int half = tid & 1, row0 = tid >> 1;
    u32x4 wv[5]; u16 cv[5];
#pragma unroll
    for (int i = 0; i < 5; ++i) {
      const int k = min(row0 + 128 * i, 575);
      wv[i] = *(const u32x4*)(wmod + (size_t)k * 3456 + cg * 16 + half * 8);
      cv[i] = c[k];
    }
    float acc[8];
#pragma unroll
    for (int j = 0; j < 8; ++j) acc[j] = 0.f;
#pragma unroll
    for (int i = 0; i < 5; ++i) {
      float s = rbf(silu_f(bf2f(cv[i])));
      if (row0 + 128 * i >= 576) s = 0.f;
#pragma unroll
      for (int j = 0; j < 4; ++j) { acc[2 * j] += s * lo2f(wv[i][j]); acc[2 * j + 1] += s * hi2f(wv[i][j]); }
    }
    *(f32x4*)&red[row0 * 16 + half * 8] = f32x4{acc[0], acc[1], acc[2], acc[3]};
    *(f32x4*)&red[row0 * 16 + half * 8 + 4] = f32x4{acc[4], acc[5], acc[6], acc[7]};
    __syncthreads();
    if (w == 0) {
      const int col = l & 15, part = l >> 4;
      float s = 0.f;
#pragma unroll
      for (int i = 0; i < 32; ++i) s += red[(part * 32 + i) * 16 + col];
      s = xor_add(s, 16); s = xor_add(s, 32);
      if (l < 16) {
        const int n = cg * 16 + col, sec = n / 576;
        float v = rbf(s);
        if (sec == 1 || sec == 4) v = rbf(1.f + v);
        ((float*)(ws + WS_MOD))[n] = v;
      }
    }
    TS(0, 1);
    return;
  }
  const int b = blockIdx.x - 216;
  if (w == 0) {
    const int row = 8 * b + (l & 7), sub = l >> 3;
    u32x4 xv[9];
#pragma unroll
    for (int i = 0; i < 9; ++i) xv[i] = *(const u32x4*)(x + (size_t)row * D_ + (sub + 8 * i) * 8);
    float s = 0.f;
#pragma unroll
    for (int i = 0; i < 9; ++i)
#pragma unroll
      for (int j = 0; j < 4; ++j) s += lo2f(xv[i][j]) + hi2f(xv[i][j]);
    const float mu = red_sub(s) * (1.f / D_);
    float q = 0.f;
#pragma unroll
    for (int i = 0; i < 9; ++i)
#pragma unroll
      for (int j = 0; j < 4; ++j) { const float a = lo2f(xv[i][j]) - mu, bb = hi2f(xv[i][j]) - mu; q += a * a + bb * bb; }
    q = red_sub(q);
    const float rstd = 1.f / sqrtf(q * (1.f / D_) + LN_EPS);
    if (sub == 0) ((f32x2*)(ws + WS_STAT))[row] = f32x2{rstd, -mu * rstd};
  } else if (w == 3) {
    if (l < 28) ((unsigned*)(ws + WS_FLAG))[b * 28 + l] = 0u;
    {
      unsigned lo, hi;
      asm volatile("s_getpc_b64 s[20:21]\n\ts_add_u32 s20, s20, _Z5k_qkvPKtPc@rel32@lo+4\n\ts_addc_u32 s21, s21, _Z5k_qkvPKtPc@rel32@hi+12\n\t"
                   "s_mov_b32 %0, s20\n\ts_mov_b32 %1, s21" : "=s"(lo), "=s"(hi) :: "s20", "s21", "scc");
      const char* code = (const char*)(((unsigned long long)hi << 32) | lo);
      const char* p = code + min((b >> 3) * 64 + l, 967) * 64;
      unsigned j0;
      asm volatile("global_load_dword %0, %1, off" : "=&v"(j0) : "v"(p) : "memory");
    }
  } else {
    const int xg = b & 7, rank = b >> 3;
    char* dst = ws + WS_WQKV + (size_t)xg * 252 * 1024;
    if (w == 1) {
      if (l < 54) {
        const int i = rank * 54 + l, kg = i / 24, rem = i % 24, part = rem >> 3, hl = (rem >> 1) & 3, half = rem & 1;
        Unit u;
        u.src = wqkv + (size_t)kg * 8 * 1728 + part * 576 + (4 * xg + hl) * 18 + half * 8;
        u.ld = 1728; u.a = 4; u.b = 2; u.c = 1;
        char* p = dst + ((kg >> 2) * 14 + (part < 2 ? 2 * hl + part : 8 + hl)) * 1024 + ((kg & 3) * 16 + half * 8) * 16;
        u32x4 in[8];
        unit_load(u, in);
#pragma unroll
        for (int i2 = 0; i2 < 8; ++i2) *(u32x4*)(p + i2 * 16) = unit_col(in, i2);
      }
    } else {
      if (l < 9) {
        const int i = rank * 9 + l, kg = i >> 2, hl = i & 3;
        const u16* src = wqkv + (size_t)kg * 8 * 1728 + (4 * xg + hl) * 18 + 16;
        unsigned in[3][8];
#pragma unroll
        for (int p = 0; p < 3; ++p)
#pragma unroll
          for (int j = 0; j < 8; ++j) in[p][j] = *(const unsigned*)(src + (size_t)j * 1728 + p * 576);
        char* pq = dst + ((kg >> 2) * 14 + 12) * 1024 + (kg & 3) * 256;
#pragma unroll
        for (int p = 0; p < 3; ++p)
#pragma unroll
          for (int e = 0; e < 2; ++e) {
            u32x4 o;
#pragma unroll
            for (int m = 0; m < 4; ++m) o[m] = __builtin_amdgcn_perm(in[p][2 * m + 1], in[p][2 * m], e ? 0x07060302u : 0x05040100u);
            if (p < 2) *(u32x4*)(pq + (4 * hl + 2 * p + e) * 16) = o;
            else { *(u32x4*)(pq + 1024 + (2 * hl + e) * 16) = o; *(u32x4*)(pq + 1024 + (8 + 2 * hl + e) * 16) = u32x4{0, 0, 0, 0}; }
          }
      }
    }
  }
}

DI void ln_apply(const u32x4 (&xv)[9], f32x2 st, const float* gs, char* A, int w, int l) {
  const int sub = l >> 3;
  f32x4 gq[2][4];
  auto fetch = [&](int i, f32x4 (&q)[4]) {
    const float* p = gs + (sub + 8 * i) * 8;
    q[0] = *(const f32x4*)p; q[1] = *(const f32x4*)(p + 4); q[2] = *(const f32x4*)(p + 576); q[3] = *(const f32x4*)(p + 580);
  };
  fetch(0, gq[0]);
#pragma unroll
  for (int i = 0; i < 9; ++i) {
    if (i + 1 < 9) fetch(i + 1, gq[(i + 1) & 1]);
    SB();
    const f32x4 (&q)[4] = gq[i & 1];
    u32x4 o;
#pragma unroll
    for (int j = 0; j < 4; ++j) {
      const f32x2 gg = {q[j >> 1][(2 * j) & 3], q[j >> 1][(2 * j + 1) & 3]}, ss = {q[2 + (j >> 1)][(2 * j) & 3], q[2 + (j >> 1)][(2 * j + 1) & 3]};
      const f32x2 xx = {lo2f(xv[i][j]), hi2f(xv[i][j])};
      const f32x2 a = (xx * f32x2{st[0], st[0]} + f32x2{st[1], st[1]}) * gg + ss;
      o[j] = pk2(a[0], a[1]);
    }
    *(u32x4*)(A + (w >> 1) * 18432 + (sub + 8 * i) * 256 + ((w & 1) * 8 + (l & 7)) * 16) = o;
    SB();
  }
}

template <bool TFORM>
DI void qkv_gemm(const char* __restrict__ wt, const char* A, int s, int l, f32x4 (&a0)[4], f32x4 (&a1)[4], f32x4& at,
                 u32x4 (&wf)[6][3]) {
  constexpr int NS = 6;
  u32x4 hf[2][4];
#pragma unroll
  for (int i = 0; i < 4; ++i) hf[0][i] = *(const u32x4*)(A + ((i + s) & 3) * 18432 + l * 16);
#pragma unroll
  for (int ks = 0; ks < 18; ++ks) {
    const int kk = ks % NS;
#pragma unroll
    for (int i = 0; i < 4; ++i) {
      if (ks + 1 < 18) hf[(ks + 1) & 1][i] = *(const u32x4*)(A + ((i + s) & 3) * 18432 + (ks + 1) * 1024 + l * 16);
      if (TFORM) {
        a0[i] = mfma16(wf[kk][0], hf[ks & 1][i], a0[i]);
        a1[i] = mfma16(wf[kk][1], hf[ks & 1][i], a1[i]);
        if (i == 0) at = mfma16(wf[kk][2], hf[ks & 1][0], at);
      } else {
        a0[i] = mfma16(hf[ks & 1][i], wf[kk][0], a0[i]);
        if (i == 0) at = mfma16(hf[ks & 1][0], wf[kk][2], at);
      }
      SB();
    }
    if (ks + NS < 18) {
      wf[kk][0] = *(const u32x4*)(wt + (ks + NS) * 14336);
      if (TFORM) wf[kk][1] = *(const u32x4*)(wt + (ks + NS) * 14336 + 1024);
      wf[kk][2] = *(const u32x4*)(wt + (ks + NS) * 14336 + (TFORM ? 12 - 2 * s : 13 - 8 - s) * 1024);
    }
    SB();
  }
}

template <bool TFORM>
DI void qkv_all(const u16* __restrict__ x, char* __restrict__ ws, char* A, float* gs, const int w) {
  const int b = blockIdx.x, tid = threadIdx.x, l = tid & 63;
  const int xg = (b & 6) + ((b >> 3) & 1), r = 2 * (b >> 4) + (b & 1), s = w & 3;
  TS(1, 0);
  u32x4 xv[9];
  const int row = r * 64 + 8 * w + (l & 7);
#pragma unroll
  for (int i = 0; i < 9; ++i) xv[i] = *(const u32x4*)(x + (size_t)row * D_ + ((l >> 3) + 8 * i) * 8);
  const f32x2 st = ((const f32x2*)(ws + WS_STAT))[row];
  const int gi = min(tid, 287);
  const f32x4 gv = *(const f32x4*)((const float*)(ws + WS_MOD) + (gi < 144 ? 576 + gi * 4 : gi * 4 - 576));
  asm volatile("s_barrier" ::: "memory");
  const char* wt = ws + WS_WQKV + (size_t)xg * 252 * 1024 + (TFORM ? 2 * s : 8 + s) * 1024 + l * 16;
  u32x4 wf[6][3];
#pragma unroll
  for (int ks = 0; ks < 6; ++ks) {
    wf[ks][0] = *(const u32x4*)(wt + ks * 14336);
    if (TFORM) wf[ks][1] = *(const u32x4*)(wt + ks * 14336 + 1024);
    wf[ks][2] = *(const u32x4*)(wt + ks * 14336 + (TFORM ? 12 - 2 * s : 13 - 8 - s) * 1024);
  }
  SB();
  if (tid < 288) *(f32x4*)(gs + tid * 4) = gv;
  __syncthreads();
  TS(1, 7);
  ln_apply(xv, st, gs, A, w, l);
  TS(1, 1);
  __syncthreads();
  TS(1, 2);
  f32x4 a0[4], a1[4], at = {0, 0, 0, 0};
#pragma unroll
  for (int i = 0; i < 4; ++i) { a0[i] = at; a1[i] = at; }
  const int g = l >> 4, n = l & 15;
  const int head = 4 * xg + s;
  char* kvh = ws + WS_KV + (size_t)head * KV_HEAD;
  qkv_gemm<TFORM>(wt, A, s, l, a0, a1, at, wf);
  TS(1, 3);
  if (TFORM) {
    char* qlo = ws + WS_QLO + (size_t)head * 65536;
#pragma unroll
    for (int i = 0; i < 4; i += 2) {
      const int mb = (i + (g & 1) + s) & 3;
      const int off = (2 * r + (mb >> 1)) * 1024 + (g >> 1) * 512 + (16 * (mb & 1) + n) * 16;
      u32x2 q0, q1, k0, k1;
      q0 = u32x2{pk2(a0[i][0] * QSCALE, a0[i][1] * QSCALE), pk2(a0[i][2] * QSCALE, a0[i][3] * QSCALE)};
      q1 = u32x2{pk2(a0[i + 1][0] * QSCALE, a0[i + 1][1] * QSCALE), pk2(a0[i + 1][2] * QSCALE, a0[i + 1][3] * QSCALE)};
      k0 = u32x2{pk2(a1[i][0], a1[i][1]), pk2(a1[i][2], a1[i][3])};
      k1 = u32x2{pk2(a1[i + 1][0], a1[i + 1][1]), pk2(a1[i + 1][2], a1[i + 1][3])};
      *(u32x4*)(qlo + off) = join16(q0, q1);
      *(u32x4*)(kvh + off) = join16(k0, k1);
    }
    const int tok = r * 64 + s * 16 + n, hg = 4 * xg + g;
    ((unsigned*)(ws + WS_QHI))[hg * 2048 + tok] = pk2(at[0] * QSCALE, at[1] * QSCALE);
    *(u32x2*)(ws + WS_KV + (size_t)hg * KV_HEAD + KV_KHI + tok * 8) = u32x2{pk2(at[2], at[3]), 0x00003f80u};
  } else {
#pragma unroll
    for (int i = 0; i < 4; i += 2) {
      const int mb = (i + (g >> 1) + s) & 3;
      const u32x2 v0 = u32x2{pk2(a0[i][0], a0[i][1]), pk2(a0[i][2], a0[i][3])};
      const u32x2 v1 = u32x2{pk2(a0[i + 1][0], a0[i + 1][1]), pk2(a0[i + 1][2], a0[i + 1][3])};
      *(u32x4*)(kvh + KV_VT + (2 * r + (mb >> 1)) * VT_TILE + (mb & 1) * 608 + (g & 1) * 304 + n * 16) = join32(v0, v1);
    }
    if (n < 12) {
      const int hg = 4 * xg + (n < 8 ? n >> 1 : n - 8), vr = n < 8 ? 16 + (n & 1) : 18;
      const u32x2 v = n < 8 ? u32x2{pk2(at[0], at[1]), pk2(at[2], at[3])} : u32x2{0x3f803f80u, 0x3f803f80u};
      *(u32x2*)(ws + WS_KV + (size_t)hg * KV_HEAD + KV_VT + (2 * r + (s >> 1)) * VT_TILE + (s & 1) * 608 + (g & 1) * 304 + vr * 16 + (g >> 1) * 8) = v;
    }
  }
  TS(1, 4);
  __builtin_amdgcn_s_waitcnt(0);
  if (TFORM) asm volatile("; end of role T" ::: "memory"); else asm volatile("; end of role N" ::: "memory");
}

__global__ __launch_bounds__(512, 2) void k_qkv(const u16* __restrict__ x, char* __restrict__ ws) {
  __shared__ __attribute__((aligned(16))) char A[73728];
  __shared__ __attribute__((aligned(16))) float gs[1152];
  const int w = __builtin_amdgcn_readfirstlane(threadIdx.x >> 6);
  if (w < 4) qkv_all<true>(x, ws, A, gs, w);
  else qkv_all<false>(x, ws, A, gs, w);
}

DI size_t w12_frag(int grp, int j, int step) {
  return grp < 4 ? grp * 180 + step * 5 + j : (grp < 8 ? 720 + (grp - 4) * 144 + step * 4 + j : j * 180 + step * 5 + 4);
}
struct RUnit { __amdgpu_buffer_rsrc_t rs; unsigned off, da, db, dc, p, s1, s2; };
DI RUnit retile_unit(int u0, int xg, int l, const u16* __restrict__ wo, const u16* __restrict__ w1, const u16* __restrict__ w2) {
  RUnit u;
  const int cl = l & 7, kgl = l >> 3;
  const unsigned w12 = (unsigned)WS_W12 + (unsigned)xg * 1296 * 1024;
  if (u0 < 81) {
    const int grp = u0 / 9, kg = (u0 % 9) * 8 + kgl, ks = kg >> 2, kq = kg & 3;
    u.rs = __builtin_amdgcn_make_buffer_rsrc((void*)w1, 0, D_ * F_ * 2, 0x00020000);
    u.off = (kg * 8 * F_ + xg * 576 + grp * 64 + cl * 8) * 2; u.da = 4 * F_ * 2; u.db = 2 * F_ * 2; u.dc = F_ * 2;
    u.p = w12 + (unsigned)w12_frag(grp, 0, ks) * 1024 + kq * 256 + cl * 16;
    u.s1 = grp < 8 ? 1024 : 180 * 1024; u.s2 = 128;
  } else if (u0 < 162) {
    const int u2 = u0 - 81, grp = u2 / 9, kg = (u2 % 9) * 8 + kgl, ks = kg >> 2, kq = kg & 3;
    const int row = (ks >> 1) * 64 + 32 * (kq & 1) + 4 * (kq >> 1) + 2 * (ks & 1);
    const int step = 18 + (ks < 8 ? ks : (ks < 16 ? ks + 2 : ks - 8));
    u.rs = __builtin_amdgcn_make_buffer_rsrc((void*)w2, 0, D_ * F_ * 2, 0x00020000);
    u.off = ((xg * 576 + row) * D_ + grp * 64 + cl * 8) * 2; u.da = D_ * 2; u.db = 16 * D_ * 2; u.dc = 8 * D_ * 2;
    if (grp < 8) { u.p = w12 + (unsigned)w12_frag(grp, 0, step) * 1024 + kq * 256 + cl * 16; u.s1 = 1024; u.s2 = 128; }
    else { u.p = w12 + (unsigned)w12_frag(8, cl >> 1, step) * 1024 + kq * 256 + (cl & 1) * 128; u.s1 = 16; u.s2 = 64; }
  } else {
    const int i3 = min(xg * 648 + (u0 - 162) * 64 + l, 5183), kg = i3 / 72, cg = i3 % 72, ks = kg >> 2, kq = kg & 3;
    const int xx = kg / 9, uu = kg % 9;
    const int row = uu < 8 ? (4 * xx + (uu >> 1)) * 18 + 8 * (uu & 1) : 4 * xx * 18 + 16;
    u.rs = __builtin_amdgcn_make_buffer_rsrc((void*)wo, 0, D_ * D_ * 2, 0x00020000);
    u.off = (row * D_ + cg * 8) * 2; u.da = (uu < 8 ? 4 : 36) * D_ * 2; u.db = (uu < 8 ? 2 : 18) * D_ * 2; u.dc = D_ * 2;
    u.p = (unsigned)WS_WO + ((cg >> 1) * 18 + ks) * 1024 + (kq * 16 + (cg & 1) * 8) * 16;
    u.s1 = 16; u.s2 = 64;
  }
  return u;
}

constexpr int L_ZERO = 160256;
constexpr int L_JUNK = 163328;
constexpr int MLP_CODE_LINES = 628;
typedef __attribute__((address_space(3))) u32x4 lds_u32x4;
typedef __attribute__((address_space(3))) u32x2 lds_u32x2;
DI u32x4 ldsr4(unsigned a) { return *(const lds_u32x4*)(uintptr_t)a; }
DI u32x2 ldsr2(unsigned a) { return *(const lds_u32x2*)(uintptr_t)a; }

__global__ __launch_bounds__(1024, 4) void k_attn(const u16* __restrict__ wo, const u16* __restrict__ w1, const u16* __restrict__ w2,
                                                 char* __restrict__ ws) {
  extern __shared__ __attribute__((aligned(16))) char lds[];
  const int b = blockIdx.x, tid = threadIdx.x, l = tid & 63;
  const int w = __builtin_amdgcn_readfirstlane(tid >> 6);
  const int xg = b & 7;
  TS(2, 0);
  const int hl = (b >> 3) & 3, head = 4 * xg + hl, qb = b >> 5;
  const int r32 = l & 31, h = l >> 5;
  const int qp = w & 3, kh = w >> 2;
  const char* kvh = ws + WS_KV + (size_t)head * KV_HEAD;
  const int tq = qb * 8 + 2 * qp;
  u32x4 qla, qlb; unsigned qha, qhb;
  {
    const char* pq = ws + WS_QLO + (size_t)head * 65536 + tq * 1024 + l * 16;
    const char* ph = ws + WS_QHI + (size_t)head * 8192 + (tq * 32 + r32) * 4;
    asm volatile("global_load_dwordx4 %0, %1, off" : "=v"(qla) : "v"(pq) : "memory");
    asm volatile("global_load_dwordx4 %0, %1, off offset:1024" : "=v"(qlb) : "v"(pq) : "memory");
    asm volatile("global_load_dword %0, %1, off" : "=v"(qha) : "v"(ph) : "memory");
    asm volatile("global_load_dword %0, %1, off offset:128" : "=v"(qhb) : "v"(ph) : "memory");
  }
  const unsigned lds0 = (unsigned)(uintptr_t)lds;
#pragma unroll
  for (int G = 0; G < 2; ++G)
#pragma unroll
    for (int m = 0; m < 5; ++m) {
      const int i = w + 16 * m;
      int off;
      if (m < 2) off = ((i >> 3) * 16 + 8 * G + (i & 7)) * 1024;
      else if (i < 40) off = KV_KHI + (((i - 32) >> 1) * 16 + 8 * G) * 256 + ((i - 32) & 1) * 1024;
      else off = KV_VT + (((i - 40) / 10) * 16 + 8 * G) * VT_TILE + ((i - 40) % 10) * 1024;
      glds16(kvh + off + l * 16, (unsigned)__builtin_amdgcn_readfirstlane(lds0 + off));
    }
  if (tid < 192) *(u32x4*)(lds + L_ZERO + tid * 16) = u32x4{0, 0, 0, 0};
  asm volatile("s_waitcnt vmcnt(5) lgkmcnt(0)\n\ts_barrier" : "+v"(qla), "+v"(qlb), "+v"(qha), "+v"(qhb) :: "memory");
  TS(2, 1);
  const int rw = w & 7, ru = rw < 5 ? (b >> 3) * 5 + rw : 153 + (b >> 3);
  const bool rdo = rw < 5 ? ru < 153 : (rw == 5 && ru < 173);
  if (w >= 14) {
    unsigned lo, hi, sv;
    asm volatile("s_getpc_b64 s[20:21]\n\ts_add_u32 s20, s20, _Z5k_mlpPKtPcPt@rel32@lo+4\n\ts_addc_u32 s21, s21, _Z5k_mlpPKtPcPt@rel32@hi+12\n\t"
                 "s_mov_b32 %0, s20\n\ts_mov_b32 %1, s21" : "=s"(lo), "=s"(hi) :: "s20", "s21", "scc");
    const char* p = (const char*)(((unsigned long long)hi << 32) | lo) + min(((b >> 3) * 2 + (w - 14)) * 10 + min(l, 9), MLP_CODE_LINES - 1) * 64;
    asm volatile("s_mov_b32 %0, m0\n\ts_mov_b32 m0, %2\n\ts_nop 0\n\tglobal_load_lds_dword %1, off\n\ts_mov_b32 m0, %0"
                 : "=&s"(sv) : "v"(p), "s"(lds0 + L_JUNK) : "memory");
  } else if (w >= 8) {
    const RUnit u = retile_unit(rdo ? ru : 0, xg, l & 56, wo, w1, w2);
    const unsigned off = u.off + u.da * ((l >> 2) & 1) + u.db * ((l >> 1) & 1) + u.dc * (l & 1);
    unsigned sv;
    asm volatile("s_mov_b32 %0, m0\n\ts_mov_b32 m0, %3\n\ts_nop 0\n\tbuffer_load_dword %1, %2, 0 offen lds\n\ts_mov_b32 m0, %0"
                 : "=&s"(sv) : "v"(off), "s"(u.rs), "s"(lds0 + L_JUNK) : "memory");
  }
  SB();
  const bool vz = r32 > 18;
  unsigned ak = lds0 + l * 16, ah = lds0 + KV_KHI + r32 * 8, av = lds0 + KV_VT + h * 304 + r32 * 16;
  const unsigned sv2 = vz ? 0u : (unsigned)(2 * VT_TILE), sh2 = h ? 0u : 512u;
  u32x2 qa = u32x2{h == 0 ? qha : 0u, 0u}, qb2 = u32x2{h == 0 ? qhb : 0u, 0u};
  const f32x16 zero16 = {0, 0, 0, 0, 0, 0, 0, 0, 0, 0, 0, 0, 0, 0, 0, 0};
  auto rowmax = [&](const f32x16& s0) {
    float m = s0[0];
#pragma unroll
    for (int i = 1; i < 16; ++i) m = fmaxf(m, s0[i]);
    auto rr = __builtin_amdgcn_permlane32_swap(__float_as_uint(m), __float_as_uint(m), false, false);
    return fmaxf(__uint_as_float(rr[0]), __uint_as_float(rr[1]));
  };
  {
    const u32x4 kl0 = ldsr4(ak);
    const u32x2 kh0 = ldsr2(ah);
    f32x16 s0 = mfma32k8(kh0, qa, mfma32(kl0, qla, zero16));
    f32x16 s1 = mfma32k8(kh0, qb2, mfma32(kl0, qlb, zero16));
    const float ma = rowmax(s0), mb = rowmax(s1);
    qa[1] = h == 0 ? (pk2(-ma, 0.f) & 0xffffu) : 0u;
    qb2[1] = h == 0 ? (pk2(-mb, 0.f) & 0xffffu) : 0u;
  }
  ak += kh * 16 * 1024; ah += kh * 16 * 256; av += kh * 16 * VT_TILE;
  if (vz) av = lds0 + L_ZERO;
  if (h) ah = lds0 + L_ZERO;
  asm volatile("" : "+v"(ak), "+v"(ah), "+v"(av));
  u32x4 kl = ldsr4(ak), v0 = ldsr4(av), v1 = ldsr4(av + 608);
  u32x2 kt = ldsr2(ah);
  f32x16 sa = mfma32k8(kt, qa, mfma32(kl, qla, zero16)), sb;
  f32x16 oa = zero16, ob = zero16;
  auto phase = [&](const f32x16& sc, f32x16& sn, f32x16& o, const u32x4& qn, const u32x2& qt, auto first, auto next) {
    constexpr int N = decltype(next)::value;
    constexpr bool F = decltype(first)::value;
    float e[16];
    u32x4 p0, p1;
#pragma unroll
    for (int i = 0; i < 5; ++i) e[i] = fast_exp2(sc[i]);
    SB();
    sn = mfma32(kl, qn, zero16);
    if (F) kl = ldsr4(ak + N * 1024);
    SB();
#pragma unroll
    for (int i = 5; i < 8; ++i) e[i] = fast_exp2(sc[i]);
#pragma unroll
    for (int j2 = 0; j2 < 4; ++j2) p0[j2] = pk2(e[2 * j2], e[2 * j2 + 1]);
    SB();
    o = mfma32(v0, p0, o);
    if (!F) v0 = ldsr4(av + N * VT_TILE);
    SB();
#pragma unroll
    for (int i = 8; i < 13; ++i) e[i] = fast_exp2(sc[i]);
    SB();
    sn = mfma32k8(kt, qt, sn);
    if (F) kt = ldsr2(ah + N * 256);
    SB();
#pragma unroll
    for (int i = 13; i < 16; ++i) e[i] = fast_exp2(sc[i]);
#pragma unroll
    for (int j2 = 0; j2 < 4; ++j2) p1[j2] = pk2(e[8 + 2 * j2], e[8 + 2 * j2 + 1]);
    SB();
    o = mfma32(v1, p1, o);
    if (!F) v1 = ldsr4(av + N * VT_TILE + 608);
    SB();
  };
#pragma unroll
  for (int G = 0; G < 2; ++G) {
    if (G == 1) {
      if (w < 8) asm volatile("s_waitcnt vmcnt(0)\n\ts_barrier" ::: "memory");
      else asm volatile("s_waitcnt vmcnt(1)\n\ts_barrier" ::: "memory");
      if (w < 8 && rdo) {
        const RUnit u = retile_unit(ru, xg, l, wo, w1, w2);
        u32x4 rin[8];
#pragma unroll
        for (int j = 0; j < 8; ++j)
          rin[j] = __builtin_amdgcn_raw_buffer_load_b128(u.rs, u.off + u.da * (j >> 2) + u.db * ((j >> 1) & 1) + u.dc * (j & 1), 0, 0);
        const __amdgpu_buffer_rsrc_t rs = ws_rsrc(ws);
        if (rw < 5) {
#pragma unroll
          for (int i = 0; i < 8; ++i) st_dev(rs, u.p + u.s1 * (i & 3) + u.s2 * (i >> 2), unit_col(rin, i));
        } else if (ru < 172 || l < 8) {
#pragma unroll
          for (int i = 0; i < 8; ++i) __builtin_amdgcn_raw_buffer_store_b128(unit_col(rin, i), rs, u.p + u.s1 * (i & 3) + u.s2 * (i >> 2), 0, 0);
        }
      }
    }
    TS(2, 2 + G);
    using N1 = std::integral_constant<int, 1>;
    using N2 = std::integral_constant<int, 2>;
    auto one_tile = [&]() {
      phase(sa, sb, oa, qlb, qb2, std::true_type{}, N1{});
      phase(sb, sa, ob, qla, qa, std::false_type{}, N1{});
      ak += 1024; ah += sh2 >> 1; av += sv2 >> 1;
      asm volatile("" : "+v"(ak), "+v"(ah), "+v"(av));
    };
    if (G == 1) { __builtin_amdgcn_s_setprio(3); one_tile(); }
#pragma unroll 1
    for (int q = 0; q < (G == 0 ? 3 : 4); ++q) {
      if (q == 0) __builtin_amdgcn_s_setprio(3);
      else if (q == 1) __builtin_amdgcn_s_setprio(2);
      else if (q == 2) __builtin_amdgcn_s_setprio(1);
      else __builtin_amdgcn_s_setprio(0);
      phase(sa, sb, oa, qlb, qb2, std::true_type{}, N1{});
      phase(sb, sa, ob, qla, qa, std::false_type{}, N1{});
      phase(sa, sb, oa, qlb, qb2, std::true_type{}, N2{});
      phase(sb, sa, ob, qla, qa, std::false_type{}, N2{});
      asm volatile("v_add_u32 %0, 0x800, %0" : "+v"(ak));
      ah += sh2; av += sv2;
      asm volatile("" : "+v"(ak), "+v"(ah), "+v"(av));
    }
    if (G == 0) { __builtin_amdgcn_s_setprio(0); one_tile(); }
  }
  TS(2, 6);
  asm volatile("s_waitcnt vmcnt(0)\n\ts_nop 15\n\ts_nop 3\n\ts_barrier" ::: "memory");
  {
    const unsigned exb = (unsigned)__builtin_amdgcn_readfirstlane(lds0 + (w & 7) * 5632);
#define EXW(o, OFF) do { unsigned msv; \
    asm volatile("s_mov_b32 %0, m0\n\ts_mov_b32 m0, %12\n\ts_nop 0\n\t" \
                 "ds_write_addtid_b32 %1 offset:%13\n\tds_write_addtid_b32 %2 offset:%13+256\n\tds_write_addtid_b32 %3 offset:%13+512\n\t" \
                 "ds_write_addtid_b32 %4 offset:%13+768\n\tds_write_addtid_b32 %5 offset:%13+1024\n\tds_write_addtid_b32 %6 offset:%13+1280\n\t" \
                 "ds_write_addtid_b32 %7 offset:%13+1536\n\tds_write_addtid_b32 %8 offset:%13+1792\n\tds_write_addtid_b32 %9 offset:%13+2048\n\t" \
                 "ds_write_addtid_b32 %10 offset:%13+2304\n\tds_write_addtid_b32 %11 offset:%13+2560\n\ts_mov_b32 m0, %0" \
                 : "=&s"(msv) : "v"(o[0]), "v"(o[1]), "v"(o[2]), "v"(o[3]), "v"(o[4]), "v"(o[5]), "v"(o[6]), "v"(o[7]), "v"(o[8]), "v"(o[9]), \
                   "v"(o[10]), "s"(exb), "n"(OFF) : "memory"); } while (0)
    if (kh == 0) EXW(ob, 2816);
    else if (kh == 1) EXW(oa, 0);
    else { EXW(oa, 45056); EXW(ob, 45056 + 2816); }
#undef EXW
  }
  asm volatile("s_waitcnt lgkmcnt(0)\n\ts_barrier" ::: "memory");
  if (kh >= 2) return;
  f32x16 o = kh ? ob : oa;
#pragma unroll
  for (int q = 1; q < 4; ++q) {
    const float* ex = (const float*)lds + ((((kh + q) & 3) * 4 + qp) * 22 + kh * 11) * 64 + l;
#pragma unroll
    for (int i = 0; i < 11; ++i) o[i] += ex[i * 64];
  }
  {
    bool bad = false;
#pragma unroll
    for (int i = 0; i < 11; ++i) bad |= !(fabsf(o[i]) < 3.0e38f);
    if (__any(bad)) {
      const u32x4 ql = kh ? qlb : qla;
      const u32x2 qt = u32x2{kh ? qb2[0] : qa[0], 0u};
      float m = -3.0e38f;
      o = zero16;
      for (int t = 0; t < 64; ++t) {
        const u32x4 gkl = *(const u32x4*)(kvh + t * 1024 + l * 16);
        const u32x2 gkh = *(const u32x2*)(kvh + KV_KHI + t * 256 + r32 * 8);
        const char* pv = kvh + KV_VT + t * VT_TILE + h * 304 + min(r32, 18) * 16;
        const u32x4 gv0 = *(const u32x4*)pv, gv1 = *(const u32x4*)(pv + 608);
        const f32x16 s = mfma32k8(gkh, qt, mfma32(gkl, ql, zero16));
        const float mn = fmaxf(m, rowmax(s)), al = fast_exp2(m - mn);
        m = mn;
#pragma unroll
        for (int i = 0; i < 16; ++i) o[i] *= al;
        u32x4 p0, p1;
#pragma unroll
        for (int j2 = 0; j2 < 4; ++j2) {
          p0[j2] = pk2(fast_exp2(s[2 * j2] - m), fast_exp2(s[2 * j2 + 1] - m));
          p1[j2] = pk2(fast_exp2(s[8 + 2 * j2] - m), fast_exp2(s[8 + 2 * j2 + 1] - m));
        }
        o = mfma32(gv0, p0, o);
        o = mfma32(gv1, p1, o);
      }
    }
  }
  auto lrow = __builtin_amdgcn_permlane32_swap(__float_as_uint(o[10]), __float_as_uint(o[10]), false, false);
  const float inv = 1.f / __uint_as_float(lrow[0]);
  const unsigned a0 = pk2(o[0] * inv, o[1] * inv), a1 = pk2(o[2] * inv, o[3] * inv);
  const unsigned b0 = pk2(o[4] * inv, o[5] * inv), b1 = pk2(o[6] * inv, o[7] * inv);
  const unsigned tail = pk2(o[8] * inv, o[9] * inv);
  auto r0 = __builtin_amdgcn_permlane32_swap(a0, b0, false, false);
  auto r1 = __builtin_amdgcn_permlane32_swap(a1, b1, false, false);
  const int tok = (tq + kh) * 32 + r32, mb = tok >> 4, row = tok & 15;
  char* ot = ws + WS_OT + (size_t)mb * 18432 + row * 16;
  const int bi = xg * 9 + 2 * hl + h, bt = xg * 9 + 8;
  *(u32x4*)(ot + (bi >> 2) * 1024 + (bi & 3) * 256) = u32x4{r0[0], r1[0], r0[1], r1[1]};
  if (h == 0) *(unsigned*)(ot + (bt >> 2) * 1024 + (bt & 3) * 256 + hl * 4) = tail;
  TS(2, 7);
}

constexpr int L_H = 0, L_X = 73728, L_STASH = 147456, L_GS = 156672, L_SYNC = 161280, L_MLP = 161344;

constexpr int MLP_NS0 = 3;
template <int NB> constexpr int mlp_ns() { return NB == 5 ? 5 : 8; }
template <int NB, int P0, int P1, int PEND, int POFF, bool BMAP>
DI void mlp_gemm(__amdgpu_buffer_rsrc_t rs, unsigned wv, unsigned a, u32x4 (&wf)[mlp_ns<NB>()][NB], f32x4 (&acc)[4][NB]) {
  constexpr int NS = mlp_ns<NB>();
  auto kmap = [](int p) { return BMAP ? (p < 8 ? p : (p < 10 ? p + 8 : p - 2)) : p; };
  unsigned a3 = a + 3 * 18432;
  asm volatile("" : "+v"(a), "+v"(a3));
  auto hload = [&](int mb, int p) { return mb < 3 ? ldsr4(a + mb * 18432 + kmap(p) * 1024) : ldsr4(a3 + kmap(p) * 1024); };
  auto refill = [&](int p, int nb) {
    if (p + NS < PEND)
      wf[(p + POFF) % NS][nb] = __builtin_amdgcn_raw_buffer_load_b128(rs, wv + (nb & 3) * 1024, ((p + POFF + NS) * NB + (nb & 4)) * 1024, 0);
  };
  u32x4 hf[2][4];
#pragma unroll
  for (int mb = 0; mb < 4; ++mb) hf[P0 & 1][mb] = hload(mb, P0);
#pragma unroll
  for (int p = P0; p < P1; ++p) {
#pragma unroll
    for (int nb = 0; nb < NB; ++nb) {
      const u32x4 wc = wf[(p + POFF) % NS][nb];
      SB();
      acc[0][nb] = mfma16(wc, hf[p & 1][0], acc[0][nb]);
      SB();
      if (nb > 0) refill(p, nb - 1); else if (p > P0) refill(p - 1, NB - 1);
      SB();
      acc[1][nb] = mfma16(wc, hf[p & 1][1], acc[1][nb]);
      SB();
      if (nb < 2 && p + 1 < P1) hf[(p + 1) & 1][2 * nb] = hload(2 * nb, p + 1);
      SB();
      acc[2][nb] = mfma16(wc, hf[p & 1][2], acc[2][nb]);
      SB();
      if (nb < 2 && p + 1 < P1) hf[(p + 1) & 1][2 * nb + 1] = hload(2 * nb + 1, p + 1);
      SB();
      acc[3][nb] = mfma16(wc, hf[p & 1][3], acc[3][nb]);
      SB();
    }
  }
  refill(P1 - 1, NB - 1);
  SB();
}

template <int NB>
DI void mlp_body(char* __restrict__ ws, char* lds, unsigned wv, u32x4 (&wf)[mlp_ns<NB>()][NB], int xg, int r, int s, int l) {
  const __amdgpu_buffer_rsrc_t rs = ws_rsrc(ws);
  const unsigned la = (unsigned)(uintptr_t)lds + l * 16;
  const int g = l >> 4, n = l & 15;
  f32x4 acc[4][NB];
#pragma unroll
  for (int mb = 0; mb < 4; ++mb)
#pragma unroll
    for (int nb = 0; nb < NB; ++nb) acc[mb][nb] = f32x4{0, 0, 0, 0};
  if (NB == 5) __builtin_amdgcn_s_setprio(2);
  mlp_gemm<NB, 0, 18, 36, 0, false>(rs, wv, la + L_H, wf, acc);
  TS(4, 5);
  if (NB == 4) asm volatile("s_barrier" ::: "memory");
  {
    char* X = lds + L_X;
    const int grp = NB == 5 ? s : 4 + s;
#pragma unroll
    for (int mb = 0; mb < 4; ++mb) {
      u32x2 xp[NB];
#pragma unroll
      for (int nb = 0; nb < NB; ++nb) {
        xp[nb] = u32x2{pk2(silu_f(acc[mb][nb][0]), silu_f(acc[mb][nb][1])), pk2(silu_f(acc[mb][nb][2]), silu_f(acc[mb][nb][3]))};
        acc[mb][nb] = f32x4{0, 0, 0, 0};
      }
#pragma unroll
      for (int p = 0; p < 2; ++p)
        *(u32x4*)(X + (mb * 18 + 2 * grp + p) * 1024 + l * 16) = u32x4{xp[2 * p][0], xp[2 * p][1], xp[2 * p + 1][0], xp[2 * p + 1][1]};
      if (NB == 5) *(u32x2*)(X + (mb * 18 + 16 + (s >> 1)) * 1024 + l * 16 + (s & 1) * 8) = xp[NB - 1];
    }
  }
  TS(4, 6);
  asm volatile("s_waitcnt lgkmcnt(0)\n\ts_barrier" ::: "memory");
  if (NB == 5) {
    mlp_gemm<NB, 0, 10, 18, 18, true>(rs, wv, la + L_X, wf, acc);
    asm volatile("s_barrier" ::: "memory");
    mlp_gemm<NB, 10, 18, 18, 18, true>(rs, wv, la + L_X, wf, acc);
  } else {
    mlp_gemm<NB, 0, 18, 18, 18, true>(rs, wv, la + L_X, wf, acc);
  }
  TS(4, 7);
  const unsigned pbase = (unsigned)WS_PART + (unsigned)((xg * 32 + r) * 8) * 9216u;
  const int grp = NB == 5 ? s : 4 + s;
#pragma unroll
  for (int mb = 0; mb < 4; ++mb) {
#pragma unroll
    for (int q = 0; q < 4; q += 2) {
      const u32x2 h0 = u32x2{pk2(acc[mb][0][q], acc[mb][1][q]), pk2(acc[mb][2][q], acc[mb][3][q])};
      const u32x2 h1 = u32x2{pk2(acc[mb][0][q + 1], acc[mb][1][q + 1]), pk2(acc[mb][2][q + 1], acc[mb][3][q + 1])};
      const int ch = 8 * grp + 4 * (g & 1) + q + (g >> 1);
      st_dev(rs, pbase + (2 * mb + (n >> 3)) * 9216 + ch * 128 + (n & 7) * 16, join32(h0, h1));
    }
  }
  if (NB == 5) {
#pragma unroll
    for (int mb = 0; mb < 4; mb += 2) {
      const u32x2 h0 = u32x2{pk2(acc[mb][NB - 1][0], acc[mb][NB - 1][1]), pk2(acc[mb][NB - 1][2], acc[mb][NB - 1][3])};
      const u32x2 h1 = u32x2{pk2(acc[mb + 1][NB - 1][0], acc[mb + 1][NB - 1][1]), pk2(acc[mb + 1][NB - 1][2], acc[mb + 1][NB - 1][3])};
      const int ch = 64 + 2 * s + (g >> 1);
      st_dev(rs, pbase + (2 * (mb + (g & 1)) + (n >> 3)) * 9216 + ch * 128 + (n & 7) * 16, join16(h0, h1));
    }
    __builtin_amdgcn_s_setprio(0);
  }
}

template <int NB>
DI void mlp_all(const u16* __restrict__ x, char* __restrict__ ws, u16* __restrict__ out, char* lds, const int w) {
  const int b = blockIdx.x, tid = threadIdx.x, l = tid & 63;
  const int xg = (b & 6) + ((b >> 3) & 1), r = 2 * (b >> 4) + (b & 1), s = w & 3;
  const int g = l >> 4, n = l & 15;
  gu32* cnt1 = (gu32*)(ws + WS_FLAG) + r * 64;
  gu32* tmo = (gu32*)(ws + WS_FLAG) + 96 * 64;
  const __amdgpu_buffer_rsrc_t rs = ws_rsrc(ws);
  TS(4, 0);
  const unsigned wv = (unsigned)WS_W12 + (unsigned)(xg * 1296 + (NB == 5 ? s * 180 : 720 + s * 144)) * 1024u + l * 16;
  u32x4 wf[mlp_ns<NB>()][NB];
  const int rh = (xg >> 1) & 1, cq = 2 * (xg >> 2) + (xg & 1);
  const float* mod = (const float*)(ws + WS_MOD);
  {
    const int gi = min(tid, 287);
    const f32x4 gv = *(const f32x4*)(mod + (gi < 144 ? 4 * 576 + gi * 4 : 3 * 576 + gi * 4 - 576));
    SB();
    char* O = lds + L_X;
    const char* src = ws + WS_OT + (size_t)(4 * r + 2 * rh) * 18432 + l * 16;
#pragma unroll
    for (int i = 0; i < 5; ++i) {
      const int pc = min(w + 8 * i, 35);
      glds16(src + pc * 1024, (unsigned)__builtin_amdgcn_readfirstlane((unsigned)(uintptr_t)O + pc * 1024));
    }
    asm volatile("s_barrier" ::: "memory");
    u32x4 wo[18], wo8[3];
    const char* wot = ws + WS_WO + (size_t)(cq * 9 * 18) * 1024 + l * 16;
#pragma unroll
    for (int i = 0; i < (NB == 5 ? 3 : 2); ++i) wo8[i] = *(const u32x4*)(wot + (8 * 18 + min(w + 8 * i, 17)) * 1024);
#pragma unroll
    for (int ks = 0; ks < 12; ++ks) wo[ks] = *(const u32x4*)(wot + (w * 18 + ks) * 1024);
    SB();
    if (tid < 288) *(f32x4*)(lds + L_GS + tid * 16) = gv;
    if (tid >= 504) *(unsigned*)(lds + L_SYNC + (tid - 504) * 4) = 0u;
    asm volatile("s_waitcnt vmcnt(14) lgkmcnt(0)\n\ts_barrier" ::: "memory");
#pragma unroll
    for (int ks = 12; ks < 18; ++ks) wo[ks] = *(const u32x4*)(wot + (w * 18 + ks) * 1024);
    u32x2 xr[2];
#pragma unroll
    for (int mb = 0; mb < 2; ++mb) xr[mb] = *(const u32x2*)(x + (size_t)(r * 64 + rh * 32 + mb * 16 + n) * D_ + cq * 144 + w * 16 + 4 * g);
    const f32x4 gav = *(const f32x4*)(mod + 2 * 576 + cq * 144 + w * 16 + 4 * g);
    u32x2 xr8[2]; f32x4 gav8;
    if (NB == 5) {
#pragma unroll
      for (int mb = 0; mb < 2; ++mb) xr8[mb] = *(const u32x2*)(x + (size_t)(r * 64 + rh * 32 + mb * 16 + n) * D_ + cq * 144 + 128 + 4 * g);
      gav8 = *(const f32x4*)(mod + 2 * 576 + cq * 144 + 128 + 4 * g);
    }
    SB();
    TS(4, 1);
    f32x4 acc[2] = {{0, 0, 0, 0}, {0, 0, 0, 0}}, a8[2] = {{0, 0, 0, 0}, {0, 0, 0, 0}};
    unsigned oa = (unsigned)(uintptr_t)O + l * 16;
    asm volatile("" : "+v"(oa));
    u32x4 o8[3][2], of[4][2];
#pragma unroll
    for (int i = 0; i < 3; ++i)
#pragma unroll
      for (int mb = 0; mb < 2; ++mb) if (i < 2 || NB == 5) o8[i][mb] = ldsr4(oa + min(w + 8 * i, 17) * 1024 + mb * 18432);
#pragma unroll
    for (int ks = 0; ks < 3; ++ks)
#pragma unroll
      for (int mb = 0; mb < 2; ++mb) of[ks][mb] = ldsr4(oa + (mb * 18 + ks) * 1024);
    SB();
#pragma unroll
    for (int i = 0; i < 3; ++i) {
      if (i < 2 || (NB == 5 && w < 2)) {
#pragma unroll
        for (int mb = 0; mb < 2; ++mb) a8[mb] = mfma16(wo8[i], o8[i][mb], a8[mb]);
      }
    }
#pragma unroll
    for (int mb = 0; mb < 2; ++mb) *(f32x4*)(O + 36864 + (w * 2 + mb) * 1024 + l * 16) = a8[mb];
    SB();
#pragma unroll
    for (int ks = 0; ks < 18; ++ks) {
      if (ks + 3 < 18) {
#pragma unroll
        for (int mb = 0; mb < 2; ++mb) of[(ks + 3) & 3][mb] = ldsr4(oa + (mb * 18 + ks + 3) * 1024);
      }
      SB();
#pragma unroll
      for (int mb = 0; mb < 2; ++mb) acc[mb] = mfma16(wo[ks], of[ks & 3][mb], acc[mb]);
      SB();
    }
    auto x1pk = [&](const f32x4& a, const u32x2& xv, const f32x4& gg) {
      return u32x2{pk2(lo2f(xv[0]) + gg[0] * a[0], hi2f(xv[0]) + gg[1] * a[1]), pk2(lo2f(xv[1]) + gg[2] * a[2], hi2f(xv[1]) + gg[3] * a[3])};
    };
    const unsigned x1b = (unsigned)WS_X1 + r * 73728u + (2 * rh + (g & 1)) * 18432u + n * 16;
    st_dev(rs, x1b + (cq * 18 + 2 * w + (g >> 1)) * 256, join16(x1pk(acc[0], xr[0], gav), x1pk(acc[1], xr[1], gav)));
    asm volatile("s_waitcnt lgkmcnt(0)\n\ts_barrier" ::: "memory");
    if (NB == 5 && w == 0) {
#pragma unroll
      for (int mb = 0; mb < 2; ++mb) {
        a8[mb] = f32x4{0, 0, 0, 0};
#pragma unroll
        for (int i = 0; i < 8; ++i) a8[mb] += *(const f32x4*)(O + 36864 + (i * 2 + mb) * 1024 + l * 16);
      }
      st_dev(rs, x1b + (cq * 18 + 16 + (g >> 1)) * 256, join16(x1pk(a8[0], xr8[0], gav8), x1pk(a8[1], xr8[1], gav8)));
    }
    asm volatile("s_barrier" ::: "memory");
    SB();
#pragma unroll
    for (int q = 0; q < MLP_NS0 * NB; ++q) wf[q / NB][q % NB] = __builtin_amdgcn_raw_buffer_load_b128(rs, wv + (q & 3) * 1024, (q & ~3) * 1024, 0);
    SB();
  }
  TS(4, 2);
  static_assert(MLP_NS0 == 3, "counted wait");
  if (NB == 5) asm volatile("s_waitcnt vmcnt(15)\n\ts_barrier" ::: "memory"); else asm volatile("s_waitcnt vmcnt(12)\n\ts_barrier" ::: "memory");
  if (tid == 0) __hip_atomic_fetch_add(cnt1, 1u, __ATOMIC_RELAXED, __HIP_MEMORY_SCOPE_AGENT);
  await(cnt1, 8, tmo);
  TS(4, 3);
  {
    u32x4 xv[9];
    const unsigned src = (unsigned)WS_X1 + r * 73728u + (w >> 1) * 18432u + ((w & 1) * 8 + (l & 7)) * 16 + (l >> 3) * 256;
#pragma unroll
    for (int i = 0; i < 9; ++i) xv[i] = ld_dev(rs, src + i * 2048);
    SB();
#pragma unroll
    for (int p = MLP_NS0; p < mlp_ns<NB>(); ++p)
#pragma unroll
      for (int nb = 0; nb < NB; ++nb) wf[p][nb] = __builtin_amdgcn_raw_buffer_load_b128(rs, wv + (nb & 3) * 1024, (p * NB + (nb & 4)) * 1024, 0);
    SB();
    float sm = 0.f, sq = 0.f;
#pragma unroll
    for (int i = 0; i < 9; ++i)
#pragma unroll
      for (int j = 0; j < 4; ++j) { sm = dot2(xv[i][j], 0x3f803f80u, sm); sq = dot2(xv[i][j], xv[i][j], sq); }
    if (w == xg) {
#pragma unroll
      for (int i = 0; i < 9; ++i) *(u32x4*)(lds + L_STASH + ((l >> 3) + 8 * i) * 128 + (l & 7) * 16) = xv[i];
    }
    const float mu = red_sub(sm) * (1.f / D_);
    const float var = fmaxf(red_sub(sq) * (1.f / D_) - mu * mu, 0.f);
    const float rstd = 1.f / sqrtf(var + LN_EPS);
    ln_apply(xv, f32x2{rstd, -mu * rstd}, (const float*)(lds + L_GS), lds + L_H, w, l);
  }
  __syncthreads();
  TS(4, 4);
  mlp_body<NB>(ws, lds, wv, wf, xg, r, s, l);
  TS(4, 8);
  const int tl = tid & 255;
  constexpr int NC = NB == 5 ? 2 : 1;
  int chunk[NC];
  f32x4 gbv[NC][2];
#pragma unroll
  for (int c = 0; c < NC; ++c) {
    chunk[c] = NB == 5 ? (c == 0 ? tl : 512 + (tl & 63)) : 256 + tl;
    gbv[c][0] = *(const f32x4*)(mod + 5 * 576 + (chunk[c] >> 3) * 8);
    gbv[c][1] = *(const f32x4*)(mod + 5 * 576 + (chunk[c] >> 3) * 8 + 4);
  }
  {
    gu32* cnt2 = (gu32*)(ws + WS_FLAG) + (32 + r + (NB == 5 ? 0 : 32)) * 64;
    typedef __attribute__((address_space(3))) unsigned lu32;
    lu32* sync = (lu32*)(uintptr_t)((unsigned)(uintptr_t)lds + L_SYNC + (NB == 5 ? 0 : 16));
    asm volatile("s_waitcnt vmcnt(0)" ::: "memory");
    unsigned old = 0;
    if (l == 0) old = __hip_atomic_fetch_add(sync, 1u, __ATOMIC_RELAXED, __HIP_MEMORY_SCOPE_WORKGROUP);
    old = __builtin_amdgcn_readfirstlane(old);
    if (l == 0) {
      if (old == 3) {
        __hip_atomic_fetch_add(cnt2, 1u, __ATOMIC_RELAXED, __HIP_MEMORY_SCOPE_AGENT);
        for (unsigned spins = 0; __hip_atomic_load(cnt2, __ATOMIC_RELAXED, __HIP_MEMORY_SCOPE_AGENT) < 8;) {
          __builtin_amdgcn_s_sleep(1);
          if (++spins > (1u << 20)) { __hip_atomic_store(tmo, 1u, __ATOMIC_RELAXED, __HIP_MEMORY_SCOPE_AGENT); break; }
        }
        __hip_atomic_store(sync + 1, 1u, __ATOMIC_RELAXED, __HIP_MEMORY_SCOPE_WORKGROUP);
      } else {
        for (unsigned spins = 0; __hip_atomic_load(sync + 1, __ATOMIC_RELAXED, __HIP_MEMORY_SCOPE_WORKGROUP) == 0 && spins < (1u << 24); ++spins)
          __builtin_amdgcn_s_sleep(2);
      }
    }
    __builtin_amdgcn_fence(__ATOMIC_ACQUIRE, "wavefront");
    asm volatile("" ::: "memory");
  }
  TS(4, 9);
#pragma unroll
  for (int c = 0; c < NC; ++c) {
    if (c == 1 && w != 0) break;
    u32x4 pp[8];
    const unsigned pb = (unsigned)WS_PART + (unsigned)(r * 8 + xg) * 9216u + chunk[c] * 16;
#pragma unroll
    for (int sl = 0; sl < 8; ++sl) pp[sl] = ld_dev(rs, pb + sl * (32 * 8 * 9216u));
    SB();
    const u32x4 xv = *(const u32x4*)(lds + L_STASH + chunk[c] * 16);
    u32x4 o;
#pragma unroll
    for (int j = 0; j < 4; ++j) {
      float a = 0.f, cc = 0.f;
#pragma unroll
      for (int sl = 0; sl < 8; ++sl) { a += lo2f(pp[sl][j]); cc += hi2f(pp[sl][j]); }
      o[j] = pk2(lo2f(xv[j]) + gbv[c][j >> 1][(2 * j) & 3] * a, hi2f(xv[j]) + gbv[c][j >> 1][(2 * j + 1) & 3] * cc);
    }
    *(u32x4*)(out + (size_t)(r * 64 + xg * 8 + (chunk[c] & 7)) * D_ + (chunk[c] >> 3) * 8) = o;
  }
  TS(4, 10);
  __builtin_amdgcn_s_waitcnt(0);
  if (NB == 5) asm volatile("; end of role A" ::: "memory"); else asm volatile("; end of role B" ::: "memory");
}

__global__ __launch_bounds__(512, 2) void k_mlp(const u16* __restrict__ x, char* __restrict__ ws, u16* __restrict__ out) {
  extern __shared__ __attribute__((aligned(16))) char lds[];
  const int w = __builtin_amdgcn_readfirstlane(threadIdx.x >> 6);
  if (w < 4) mlp_all<5>(x, ws, out, lds, w);
  else mlp_all<4>(x, ws, out, lds, w);
}

extern "C" void kernel_launch(void* const* d_in, const int* in_sizes, int n_in, void* d_out, int out_size, void* d_ws, size_t ws_size,
                              hipStream_t stream) {
  const u16* x = (const u16*)d_in[0];
  const u16* wqkv = (const u16*)d_in[1];
  const u16* wo = (const u16*)d_in[2];
  const u16* w1 = (const u16*)d_in[4];
  const u16* w2 = (const u16*)d_in[5];
  const u16* c = (const u16*)d_in[6];
  const u16* wmod = (const u16*)d_in[7];
  char* ws = (char*)d_ws;
  (void)hipFuncSetAttribute((const void*)k_attn, hipFuncAttributeMaxDynamicSharedMemorySize, 163840);
  (void)hipFuncSetAttribute((const void*)k_mlp, hipFuncAttributeMaxDynamicSharedMemorySize, L_MLP);
  k_prep<<<472, 256, 0, stream>>>(x, wqkv, c, wmod, ws);
  k_qkv<<<256, 512, 0, stream>>>(x, ws);
  k_attn<<<256, 1024, 163840, stream>>>(wo, w1, w2, ws);
  k_mlp<<<256, 512, L_MLP, stream>>>(x, ws, (u16*)d_out);
}
```
